```python
import torch

def _softmax(x, axis=-1):
    return torch.softmax(x, dim=axis)
def _gelu(x):
    return torch.nn.functional.gelu(x, approximate="tanh")
def _silu(x):
    return torch.nn.functional.silu(x)
def _sigmoid(x):
    return torch.sigmoid(x)
def _layernorm(x, axis=-1, eps=1e-5):
    m = x.mean(dim=axis, keepdim=True)
    v = x.var(dim=axis, keepdim=True, unbiased=False)
    return (x - m) / torch.sqrt(v + eps)
def _scatter_add(tgt, idx, src, axis=0):
    return tgt.clone().index_add_(axis, idx, src)
def _segment_sum(src, seg, n):
    out = torch.zeros((n,) + src.shape[1:], dtype=src.dtype)
    return out.index_add_(0, seg, src)
def _pad(x, pw):
    flat = tuple(v for pair in reversed(pw) for v in pair)
    return torch.nn.functional.pad(x, flat)

kernel_name = 'standalone__gqa_softmax__decode'

def setup_inputs():

    _w = (lambda a: (lambda t: t.to(torch.bfloat16) if t.is_floating_point() else t)(torch.as_tensor(a)))
    B, Hq, Hkv, Sq, Sk, D = 4, 32, 16, 1, 4096, 128
    return {
        "q": _w((lambda *s: torch.randn(*s, dtype=torch.bfloat16) * 0.8)(B, Hq, Sq, D)),
        "k": _w((lambda *s: torch.randn(*s, dtype=torch.bfloat16) * 0.8)(B, Hkv, Sk, D)),
        "v": _w((lambda *s: torch.randn(*s, dtype=torch.bfloat16) * 0.05)(B, Hkv, Sk, D)),
    }

def reference(q, k, v):
    B, Hq, Sq, D = q.shape
    Hkv = k.shape[1]
    rep = Hq // Hkv
    k = (lambda x, n, axis: torch.repeat_interleave(x, n, dim=axis))(k, rep, axis=1)
    v = (lambda x, n, axis: torch.repeat_interleave(x, n, dim=axis))(v, rep, axis=1)
    scale = D ** -0.5
    scores = torch.einsum("bhqd,bhkd->bhqk", q, k) * scale
    p = _softmax(scores, axis=-1)
    return torch.einsum("bhqk,bhkd->bhqd", p, v)
```

```cpp
#include <hip/hip_runtime.h>
#include <hip/hip_bf16.h>
#include <cstdint>

#define NSPLIT 4
#define NW 16
#define VPRE 8
#define KV_NT 1
#define XMAP 1
#define FUSE 1

constexpr int Bn = 4, HQ = 32, HKV = 16, SK = 4096, D = 128, G = 2;
constexpr int NGRP = Bn * HKV;
constexpr int KPW = SK / (NSPLIT * NW);
constexpr int NLD = KPW / 4;
constexpr int NG = NLD / 8;
static_assert(NLD % 8 == 0 && VPRE <= NLD, "");
constexpr float C2 = 0.088388347648318440f * 1.4426950408889634f;

using u32x4 = __attribute__((ext_vector_type(4))) unsigned;
using u32x2 = __attribute__((ext_vector_type(2))) unsigned;
using f32x4 = __attribute__((ext_vector_type(4))) float;
using bf16x2 = __attribute__((ext_vector_type(2))) __bf16;
using bf16x8 = __attribute__((ext_vector_type(8))) __bf16;

__device__ __forceinline__ float dot2(unsigned k, unsigned q, float acc) {
  return __builtin_amdgcn_fdot2_f32_bf16(__builtin_bit_cast(bf16x2, k), __builtin_bit_cast(bf16x2, q), acc, false);
}
template <int CTRL> __device__ __forceinline__ float dpp(float x) {
  return __builtin_bit_cast(float, __builtin_amdgcn_mov_dpp(__builtin_bit_cast(int, x), CTRL, 0xf, 0xf, true));
}
template <int CTRL> __device__ __forceinline__ unsigned dppu(unsigned x) {
  return (unsigned)__builtin_amdgcn_mov_dpp((int)x, CTRL, 0xf, 0xf, true);
}
constexpr int XOR1 = 0xB1, XOR2 = 0x4E, XOR7 = 0x141, XOR8 = 0x128, ROR4 = 0x124, BCAST = 0x150;
__device__ __forceinline__ float xrow_max(float x) {
  auto s = __builtin_amdgcn_permlane16_swap(__float_as_uint(x), __float_as_uint(x), false, false);
  x = fmaxf(__uint_as_float(s[0]), __uint_as_float(s[1]));
  auto t = __builtin_amdgcn_permlane32_swap(__float_as_uint(x), __float_as_uint(x), false, false);
  return fmaxf(__uint_as_float(t[0]), __uint_as_float(t[1]));
}
__device__ __forceinline__ float xrow_sum(float x) {
  auto s = __builtin_amdgcn_permlane16_swap(__float_as_uint(x), __float_as_uint(x), false, false);
  x = __uint_as_float(s[0]) + __uint_as_float(s[1]);
  auto t = __builtin_amdgcn_permlane32_swap(__float_as_uint(x), __float_as_uint(x), false, false);
  return __uint_as_float(t[0]) + __uint_as_float(t[1]);
}
__device__ __forceinline__ unsigned cvtpk(float lo, float hi) {
  bf16x2 r = {(__bf16)lo, (__bf16)hi};
  return __builtin_bit_cast(unsigned, r);
}
__device__ __forceinline__ u32x4 ldkv(const u32x4* p) {
  return __builtin_nontemporal_load(p);
}

constexpr unsigned WS_EPOCH = 0, WS_SLOT = 16384, SLOT_BYTES = 2304;
constexpr unsigned WS_BYTES = WS_SLOT + NGRP * NSPLIT * SLOT_BYTES;

template <int T> __device__ __forceinline__ void pv_step(f32x4& acc, const u32x4& v, unsigned pk, const u32x4& sel) {
  const unsigned z = dppu<BCAST + 2 * T>(pk);
  u32x4 b;
#pragma unroll
  for (int w = 0; w < 4; ++w) b[w] = __builtin_amdgcn_perm(z, z, sel[w]);
  acc = __builtin_amdgcn_mfma_f32_16x16x32_bf16(__builtin_bit_cast(bf16x8, b), __builtin_bit_cast(bf16x8, v), acc, 0, 0, 0);
}

__global__ void __launch_bounds__(NW * 64) attn_split(const u32x4* __restrict__ q, const u32x4* __restrict__ k,
                                                     const u32x4* __restrict__ v, unsigned char* __restrict__ ws,
                                                     u32x2* __restrict__ out) {
  __shared__ __attribute__((aligned(16))) float lds[NW * 256 + NW * 4];
  const int tid = threadIdx.x, lane = tid & 63, wave = __builtin_amdgcn_readfirstlane(tid >> 6);
  const int c = lane & 15;
  const int grp = (blockIdx.x & 7) * 8 + (blockIdx.x >> 3) / NSPLIT, split = (blockIdx.x >> 3) % NSPLIT;
  const size_t base = ((size_t)grp * SK + (size_t)(split * NW + wave) * KPW) * 16 + lane;
  const u32x4* kp = k + base;
  const u32x4* vp = v + base;
  const u32x4 q0 = q[(size_t)grp * 32 + c], q1 = q[(size_t)grp * 32 + 16 + c];
  const unsigned epoch = __hip_atomic_load(reinterpret_cast<unsigned*>(ws + WS_EPOCH + grp * 128), __ATOMIC_RELAXED, __HIP_MEMORY_SCOPE_AGENT);
  u32x4 K[NLD], V[NLD];
#pragma unroll
  for (int t = 0; t < NLD; ++t) K[t] = ldkv(kp + t * 64);
#pragma unroll
  for (int t = 0; t < VPRE; ++t) V[t] = ldkv(vp + t * 64);
  __builtin_amdgcn_sched_barrier(0);
  float s[NG];
#pragma unroll
  for (int g = 0; g < NG; ++g) {
    float x[16];
#pragma unroll
    for (int t = 0; t < 8; ++t) {
      float d0 = 0.f, d1 = 0.f;
#pragma unroll
      for (int w = 0; w < 4; ++w) { d0 = dot2(K[g * 8 + t][w], q0[w], d0); d1 = dot2(K[g * 8 + t][w], q1[w], d1); }
      x[2 * t] = d0; x[2 * t + 1] = d1;
    }
    __builtin_amdgcn_sched_barrier(0);
    constexpr int PER = (NLD - VPRE + NG - 1) / NG;
#pragma unroll
    for (int t = 0; t < PER; ++t) { const int i = VPRE + g * PER + t; if (i < NLD) V[i] = ldkv(vp + i * 64); }
    __builtin_amdgcn_sched_barrier(0);
#define TR_STEP(HALF, CTRL, BIT) _Pragma("unroll") for (int h = 0; h < (HALF); ++h) { \
      const float lo_ = x[h], hi_ = x[h + (HALF)]; const float t1_ = lo_ + dpp<CTRL>(lo_), t2_ = hi_ + dpp<CTRL>(hi_); \
      x[h] = (c & (BIT)) ? t2_ : t1_; }
    TR_STEP(8, XOR8, 8) TR_STEP(4, XOR7, 4) TR_STEP(2, XOR2, 2) TR_STEP(1, XOR1, 1)
#undef TR_STEP
    s[g] = x[0] * C2;
  }
  float m = s[0];
#pragma unroll
  for (int g = 1; g < NG; ++g) m = fmaxf(m, s[g]);
  m = fmaxf(m, dpp<XOR2>(m)); m = fmaxf(m, dpp<ROR4>(m)); m = fmaxf(m, dpp<XOR8>(m)); m = xrow_max(m);
  float l = 0.f;
  unsigned pk[NG];
#pragma unroll
  for (int g = 0; g < NG; ++g) {
    const float p = __builtin_amdgcn_exp2f(s[g] - m);
    l += p;
    pk[g] = cvtpk(p, dpp<XOR1>(p));
  }
  l += dpp<XOR2>(l); l += dpp<ROR4>(l); l += dpp<XOR8>(l); l = xrow_sum(l);
  if (lane < 2) { lds[NW * 256 + lane * 2 * NW + wave] = m; lds[NW * 256 + lane * 2 * NW + NW + wave] = l; }
  u32x4 sel;
  {
    const int h = c >> 3, j = c & 7;
    const unsigned srcb = h ? 0x0302u : 0x0100u;
    const unsigned pat = (j & 1) ? ((srcb << 16) | 0x0c0cu) : (0x0c0c0000u | srcb);
#pragma unroll
    for (int w = 0; w < 4; ++w) sel[w] = ((j >> 1) == w) ? pat : 0x0c0c0c0cu;
  }
  f32x4 acc = {0.f, 0.f, 0.f, 0.f};
#pragma unroll
  for (int g = 0; g < NG; ++g) {
    pv_step<0>(acc, V[g * 8 + 0], pk[g], sel); pv_step<1>(acc, V[g * 8 + 1], pk[g], sel);
    pv_step<2>(acc, V[g * 8 + 2], pk[g], sel); pv_step<3>(acc, V[g * 8 + 3], pk[g], sel);
    pv_step<4>(acc, V[g * 8 + 4], pk[g], sel); pv_step<5>(acc, V[g * 8 + 5], pk[g], sel);
    pv_step<6>(acc, V[g * 8 + 6], pk[g], sel); pv_step<7>(acc, V[g * 8 + 7], pk[g], sel);
  }
  *reinterpret_cast<f32x4*>(&lds[wave * 256 + lane * 4]) = acc;
  __syncthreads();
  if (wave == 0) {
    const int h = lane >> 5;
    const float* ml = lds + NW * 256 + h * 2 * NW;
    float mw[NW], lw[NW];
#pragma unroll
    for (int w = 0; w < NW; w += 4) {
      const f32x4 a = *reinterpret_cast<const f32x4*>(ml + w), b = *reinterpret_cast<const f32x4*>(ml + NW + w);
#pragma unroll
      for (int i = 0; i < 4; ++i) { mw[w + i] = a[i]; lw[w + i] = b[i]; }
    }
    float M = mw[0];
#pragma unroll
    for (int w = 1; w < NW; ++w) M = fmaxf(M, mw[w]);
    float L = 0.f;
    f32x4 o = {0.f, 0.f, 0.f, 0.f};
#pragma unroll
    for (int w = 0; w < NW; ++w) {
      const float sc = __builtin_amdgcn_exp2f(mw[w] - M);
      L = fmaf(sc, lw[w], L);
      const f32x4 a = *reinterpret_cast<const f32x4*>(&lds[w * 256 + lane * 4]);
#pragma unroll
      for (int i = 0; i < 4; ++i) o[i] = fmaf(sc, a[i], o[i]);
    }
    const unsigned tag = epoch * 2654435761u + 0x9E3779B9u;
    const auto rs = __builtin_amdgcn_make_buffer_rsrc(ws + WS_SLOT + (size_t)grp * NSPLIT * SLOT_BYTES, 0, NSPLIT * SLOT_BYTES, 0x00020000);
    if (split != 0) {
      const unsigned so = split * SLOT_BYTES;
      __builtin_amdgcn_raw_buffer_store_b128(u32x4{tag, __float_as_uint(o[0]), tag, __float_as_uint(o[1])}, rs, lane * 16, so, 16);
      __builtin_amdgcn_raw_buffer_store_b128(u32x4{tag, __float_as_uint(o[2]), tag, __float_as_uint(o[3])}, rs, lane * 16 + 1024, so, 16);
      if ((lane & 31) == 0) __builtin_amdgcn_raw_buffer_store_b128(u32x4{tag, __float_as_uint(M), tag, __float_as_uint(L)}, rs, h * 16 + 2048, so, 16);
    } else {
      u32x4 ga[NSPLIT], gb[NSPLIT], gm[NSPLIT];
      for (unsigned spin = 0; spin < (1u << 22); ++spin) {
        asm volatile("" ::: "memory");
        bool ok = true;
#pragma unroll
        for (int sp = 1; sp < NSPLIT; ++sp) {
          ga[sp] = __builtin_amdgcn_raw_buffer_load_b128(rs, lane * 16, sp * SLOT_BYTES, 16);
          gb[sp] = __builtin_amdgcn_raw_buffer_load_b128(rs, lane * 16 + 1024, sp * SLOT_BYTES, 16);
          gm[sp] = __builtin_amdgcn_raw_buffer_load_b128(rs, h * 16 + 2048, sp * SLOT_BYTES, 16);
        }
#pragma unroll
        for (int sp = 1; sp < NSPLIT; ++sp)
          ok &= (ga[sp][0] == tag) & (ga[sp][2] == tag) & (gb[sp][0] == tag) & (gb[sp][2] == tag) & (gm[sp][0] == tag) & (gm[sp][2] == tag);
        if (__all(ok)) break;
        __builtin_amdgcn_s_sleep(1);
      }
      float MM = M;
#pragma unroll
      for (int sp = 1; sp < NSPLIT; ++sp) MM = fmaxf(MM, __uint_as_float(gm[sp][1]));
      const float s0 = __builtin_amdgcn_exp2f(M - MM);
      L *= s0;
#pragma unroll
      for (int i = 0; i < 4; ++i) o[i] *= s0;
#pragma unroll
      for (int sp = 1; sp < NSPLIT; ++sp) {
        const float sc = __builtin_amdgcn_exp2f(__uint_as_float(gm[sp][1]) - MM);
        L = fmaf(sc, __uint_as_float(gm[sp][3]), L);
        o[0] = fmaf(sc, __uint_as_float(ga[sp][1]), o[0]); o[1] = fmaf(sc, __uint_as_float(ga[sp][3]), o[1]);
        o[2] = fmaf(sc, __uint_as_float(gb[sp][1]), o[2]); o[3] = fmaf(sc, __uint_as_float(gb[sp][3]), o[3]);
      }
      const float inv = 1.f / L;
      out[grp * 64 + h * 32 + 2 * c + ((lane >> 4) & 1)] = u32x2{cvtpk(o[0] * inv, o[1] * inv), cvtpk(o[2] * inv, o[3] * inv)};
      if (lane == 0) __hip_atomic_store(reinterpret_cast<unsigned*>(ws + WS_EPOCH + grp * 128), tag, __ATOMIC_RELAXED, __HIP_MEMORY_SCOPE_AGENT);
    }
  }
}

extern "C" void kernel_launch(void* const* d_in, const int* in_sizes, int n_in, void* d_out, int out_size, void* d_ws,
                              size_t ws_size, hipStream_t stream) {
  attn_split<<<NGRP * NSPLIT, NW * 64, 0, stream>>>((const u32x4*)d_in[0], (const u32x4*)d_in[1], (const u32x4*)d_in[2], (unsigned char*)d_ws, (u32x2*)d_out);
}
```

```cpp
#include <hip/hip_runtime.h>
#include <hip/hip_bf16.h>
#include <cstdint>
#include <utility>

#define WIN 6
#define PRE 0
#define NB (WIN + PRE)
#define AUXS 3
#define PW 110
#define QPOS 0
#define MLW sp
#define BAR1 (128 - WIN)

constexpr int NW = 4, NGRP = 64;
constexpr float C2 = 0.088388347648318440f * 1.4426950408889634f;

using u32x4 = __attribute__((ext_vector_type(4))) unsigned;
using u32x2 = __attribute__((ext_vector_type(2))) unsigned;
using f32x2 = __attribute__((ext_vector_type(2))) float;
using f32x4 = __attribute__((ext_vector_type(4))) float;
using bf16x2 = __attribute__((ext_vector_type(2))) __bf16;
using bf16x8 = __attribute__((ext_vector_type(8))) __bf16;

__device__ __forceinline__ float dot2(unsigned k, unsigned q, float acc) {
  return __builtin_amdgcn_fdot2_f32_bf16(__builtin_bit_cast(bf16x2, k), __builtin_bit_cast(bf16x2, q), acc, false);
}
template <int CTRL> __device__ __forceinline__ float dpp(float x) {
  return __builtin_bit_cast(float, __builtin_amdgcn_mov_dpp(__builtin_bit_cast(int, x), CTRL, 0xf, 0xf, true));
}
template <int CTRL> __device__ __forceinline__ unsigned dppu(unsigned x) {
  return (unsigned)__builtin_amdgcn_mov_dpp((int)x, CTRL, 0xf, 0xf, true);
}
constexpr int XOR1 = 0xB1, XOR2 = 0x4E, XOR7 = 0x141, XOR8 = 0x128, ROR4 = 0x124, BCAST = 0x150, EVEN = 0xA0, ODD = 0xF5;
__device__ __forceinline__ float xrow_max(float x) {
  auto s = __builtin_amdgcn_permlane16_swap(__float_as_uint(x), __float_as_uint(x), false, false);
  x = fmaxf(__uint_as_float(s[0]), __uint_as_float(s[1]));
  auto t = __builtin_amdgcn_permlane32_swap(__float_as_uint(x), __float_as_uint(x), false, false);
  return fmaxf(__uint_as_float(t[0]), __uint_as_float(t[1]));
}
__device__ __forceinline__ float xrow_sum(float x) {
  auto s = __builtin_amdgcn_permlane16_swap(__float_as_uint(x), __float_as_uint(x), false, false);
  x = __uint_as_float(s[0]) + __uint_as_float(s[1]);
  auto t = __builtin_amdgcn_permlane32_swap(__float_as_uint(x), __float_as_uint(x), false, false);
  return __uint_as_float(t[0]) + __uint_as_float(t[1]);
}
__device__ __forceinline__ unsigned cvtpk(float lo, float hi) {
  bf16x2 r = {(__bf16)lo, (__bf16)hi};
  return __builtin_bit_cast(unsigned, r);
}
__device__ __forceinline__ float ex2(float x) { return __builtin_amdgcn_exp2f(x); }
__device__ __forceinline__ void wg_barrier() {
  asm volatile("s_waitcnt lgkmcnt(0)" ::: "memory");
  __builtin_amdgcn_s_barrier();
  asm volatile("" ::: "memory");
}
template <class F, int... I> __device__ __forceinline__ void sfor_(F&& f, std::integer_sequence<int, I...>) { (f(std::integral_constant<int, I>{}), ...); }
template <int N, class F> __device__ __forceinline__ void sfor(F&& f) { sfor_(f, std::make_integer_sequence<int, N>{}); }
#define SB() __builtin_amdgcn_sched_barrier(0)
#define CB() asm volatile("" ::: "memory")
#define ALL(x) (__builtin_amdgcn_ballot_w64(x) == ~0ull)

constexpr unsigned GRP_BYTES = 8192, WS_ML = 6144, WS_EP = 6272, OOB = 0x40000000u;

template <int T> __device__ __forceinline__ u32x4 frag(unsigned pk, const u32x4& sel) {
  const unsigned z = dppu<BCAST + 2 * T>(pk);
  u32x4 b;
#pragma unroll
  for (int w = 0; w < 4; ++w) b[w] = __builtin_amdgcn_perm(z, z, sel[w]);
  return b;
}
__device__ __forceinline__ void mma(f32x4& acc, const u32x4& a, const u32x4& v) {
  acc = __builtin_amdgcn_mfma_f32_16x16x32_bf16(__builtin_bit_cast(bf16x8, a), __builtin_bit_cast(bf16x8, v), acc, 0, 0, 0);
}

__global__ void __launch_bounds__(NW * 64, 2) gqa_decode(const unsigned char* __restrict__ q, const unsigned char* __restrict__ k,
                                                        const unsigned char* __restrict__ v, unsigned char* ws,
                                                        unsigned short* out) {
  __shared__ __attribute__((aligned(16))) float lds[NW * 256 + 20];
  const int tid = threadIdx.x, lane = tid & 63, wave = __builtin_amdgcn_readfirstlane(tid >> 6);
  const int c = lane & 15;
  const int grp = (blockIdx.x & 7) * 8 + (blockIdx.x >> 5), sp = (blockIdx.x >> 3) & 3;
  unsigned vo = lane * 16 + wave * 4096 + sp * 262144 + grp * 1048576;
  unsigned qo = c * 16 + grp * 512;
  asm volatile("" : "+v"(vo), "+v"(qo));
  SB();
  const auto rk = __builtin_amdgcn_make_buffer_rsrc((void*)k, 0, 64u << 20, 0x00020000);
  const auto rq = __builtin_amdgcn_make_buffer_rsrc((void*)q, 0, 32768, 0x00020000);
  u32x4 R[NB], q0, q1;
  const int h = lane >> 5;
  unsigned oo, g0, gb, tag;
  unsigned short o0;
  __amdgpu_buffer_rsrc_t rv, rw, ro;
  auto issue = [&](auto ic) {
    constexpr int I = decltype(ic)::value;
    if constexpr (I == QPOS) { q0 = __builtin_amdgcn_raw_buffer_load_b128(rq, qo, 0, 0); q1 = __builtin_amdgcn_raw_buffer_load_b128(rq, qo + 256, 0, 0); SB(); }
    if constexpr (I < 64) R[I % NB] = __builtin_amdgcn_raw_buffer_load_b128(rk, vo + (I & 3) * 1024, (I >> 2) * 16384, AUXS);
    else if constexpr (I < 128) R[I % NB] = __builtin_amdgcn_raw_buffer_load_b128(rv, vo + (I & 3) * 1024, ((I - 64) >> 2) * 16384, AUXS);
    SB();
    if constexpr (I == WIN - 1) {
      rv = __builtin_amdgcn_make_buffer_rsrc((void*)v, 0, 64u << 20, 0x00020000);
      rw = __builtin_amdgcn_make_buffer_rsrc(ws, 0, NGRP * GRP_BYTES, 0x00020000);
      ro = __builtin_amdgcn_make_buffer_rsrc(out, 0, 32768, 0x00020000);
      gb = grp * GRP_BYTES;
      oo = (grp * 256 + h * 128 + sp * 32 + (lane & 31)) * 2;
    }
    if constexpr (I == PW) {
      CB();
      const unsigned skip = wave == sp ? 0u : OOB;
      g0 = __builtin_amdgcn_raw_buffer_load_b32(rw, (lane < 12 ? sp * 1536 + lane * 128 : lane < 14 ? WS_ML + (lane - 12) * 128 : OOB) | skip, gb, 16);
      o0 = __builtin_amdgcn_raw_buffer_load_b16(ro, oo | skip, 0, 16);
      CB();
      SB();
    }
  };
  sfor<WIN>(issue);
  float s[8];
  sfor<8>([&](auto gc) {
    constexpr int G = decltype(gc)::value;
    float x[16];
    sfor<8>([&](auto tc) {
      constexpr int T = decltype(tc)::value, I = G * 8 + T;
      float d0 = 0.f, d1 = 0.f;
      if constexpr (PRE) { asm volatile("" : "+v"(R[I % NB])); SB(); issue(std::integral_constant<int, I + WIN>{}); }
#pragma unroll
      for (int w = 0; w < 4; ++w) { d0 = dot2(R[I % NB][w], q0[w], d0); d1 = dot2(R[I % NB][w], q1[w], d1); }
      x[2 * T] = d0; x[2 * T + 1] = d1;
      SB();
      if constexpr (!PRE) issue(std::integral_constant<int, I + WIN>{});
    });
#define TR_STEP(HALF, CTRL, BIT) _Pragma("unroll") for (int h = 0; h < (HALF); ++h) { \
      const float lo_ = x[h], hi_ = x[h + (HALF)]; const float t1_ = lo_ + dpp<CTRL>(lo_), t2_ = hi_ + dpp<CTRL>(hi_); \
      x[h] = (c & (BIT)) ? t2_ : t1_; }
    TR_STEP(8, XOR8, 8) TR_STEP(4, XOR7, 4) TR_STEP(2, XOR2, 2) TR_STEP(1, XOR1, 1)
#undef TR_STEP
    s[G] = x[0] * C2;
    SB();
  });
  float m = s[0];
#pragma unroll
  for (int g = 1; g < 8; ++g) m = fmaxf(m, s[g]);
  m = fmaxf(m, dpp<XOR2>(m)); m = fmaxf(m, dpp<ROR4>(m)); m = fmaxf(m, dpp<XOR8>(m)); m = xrow_max(m);
  float l = 0.f;
  unsigned pk[8];
#pragma unroll
  for (int g = 0; g < 8; ++g) {
    const float p = ex2(s[g] - m);
    l += p;
    pk[g] = cvtpk(p, dpp<XOR1>(p));
  }
  u32x4 sel;
  {
    const int h = c >> 3, j = c & 7;
    const unsigned srcb = h ? 0x0302u : 0x0100u;
    const unsigned pat = (j & 1) ? ((srcb << 16) | 0x0c0cu) : (0x0c0c0000u | srcb);
#pragma unroll
    for (int w = 0; w < 4; ++w) sel[w] = ((j >> 1) == w) ? pat : 0x0c0c0c0cu;
  }
  SB();
  f32x4 acc = {0.f, 0.f, 0.f, 0.f};
  float* const lml = lds + NW * 256;
  float ew;
  unsigned lw, lr, xo, mo, eo;
  sfor<64>([&](auto jc) {
    constexpr int J = decltype(jc)::value, I = 64 + J;
    if constexpr (I == BAR1) {
      wg_barrier();
      const f32x4 a = *reinterpret_cast<const f32x4*>(lml + h * 8), b = *reinterpret_cast<const f32x4*>(lml + h * 8 + 4);
      const float mw = lml[h * 8 + wave];
      tag = __float_as_uint(lml[16]) * 2654435761u + 0x9E3779B9u;
      const float M = fmaxf(fmaxf(a[0], a[1]), fmaxf(a[2], a[3]));
      const float L = fmaf(ex2(a[3] - M), b[3], fmaf(ex2(a[2] - M), b[2], fmaf(ex2(a[1] - M), b[1], ex2(a[0] - M) * b[0])));
      ew = ex2(mw - M);
      __builtin_amdgcn_raw_buffer_store_b128(u32x4{tag, __float_as_uint(M), tag, __float_as_uint(L)}, rw,
                                             ((lane & 31) || wave != MLW) ? OOB : WS_ML + sp * 32 + h * 16, gb, 16);
      lw = wave * 1024 + lane * 16;
      if (wave != sp) {
        lr = (((lane & 16) * 2 + (lane & 2) * 8 + wave * 4 + ((lane >> 2) & 3)) * 4 + (lane & 1) * 2) * 4;
        xo = lane < 32 ? wave * 1536 + ((sp - wave - 1) & 3) * 512 + lane * 16 : OOB;
      } else {
        lr = ((h * 32 + (lane & 4) * 4 + wave * 4 + ((lane >> 3) & 3)) * 4 + (lane & 3)) * 4;
        xo = sp * 1536 + lane * 8;
      }
      mo = WS_ML + ((sp + 1 + lane) & 3) * 32 + h * 16;
      eo = (lane | sp) ? OOB : WS_EP;
      asm volatile("" : "+v"(ew), "+v"(tag), "+v"(lw), "+v"(lr), "+v"(xo), "+v"(mo), "+v"(eo));
      SB();
    }
    mma(acc, frag<J & 7>(pk[J >> 3], sel), R[I % NB]);
    SB();
    issue(std::integral_constant<int, I + WIN>{});
    if constexpr (I == 64 + WIN) {
      l += dpp<XOR2>(l); l += dpp<ROR4>(l); l += dpp<XOR8>(l); l = xrow_sum(l);
      if (lane < 2) { lml[lane * 8 + wave] = m; lml[lane * 8 + 4 + wave] = l; }
    }
    if constexpr (I == (PW + 1 > 64 ? PW + 1 : 64)) {
      asm volatile("" :: "v"(o0));
      lml[(wave == sp && lane == 13) ? 16 : 17] = __uint_as_float(g0);
    }
    SB();
  });
  *reinterpret_cast<f32x4*>(reinterpret_cast<char*>(lds) + lw) = acc * ew;
  wg_barrier();
  const float* src = reinterpret_cast<const float*>(reinterpret_cast<const char*>(lds) + lr);
  if (wave != sp) {
    const f32x2 o = (*(const f32x2*)src + *(const f32x2*)(src + 256)) + (*(const f32x2*)(src + 512) + *(const f32x2*)(src + 768));
    __builtin_amdgcn_raw_buffer_store_b128(u32x4{tag, __float_as_uint(o[0]), tag, __float_as_uint(o[1])}, rw, xo, gb, 16);
  } else {
    u32x2 g[3];
    u32x4 gm;
    auto ldv = [&]() {
#pragma unroll
      for (int j = 0; j < 3; ++j) g[j] = __builtin_amdgcn_raw_buffer_load_b64(rw, xo + j * 512, gb, 16);
    };
    gm = __builtin_amdgcn_raw_buffer_load_b128(rw, mo, gb, 16);
    ldv();
    CB();
    const float oe = (src[0] + src[256]) + (src[512] + src[768]);
    if (!ALL((gm[0] == tag) & (gm[2] == tag))) {
      _Pragma("unroll 1") for (unsigned spin = 0; spin < (1u << 22); ++spin) {
        CB();
        gm = __builtin_amdgcn_raw_buffer_load_b128(rw, mo, gb, 16);
        if (ALL((gm[0] == tag) & (gm[2] == tag))) break;
      }
    }
    const float Mj = __uint_as_float(gm[1]);
    float M = fmaxf(Mj, dpp<XOR1>(Mj)); M = fmaxf(M, dpp<XOR2>(M));
    const float e = ex2(Mj - M);
    float L = e * __uint_as_float(gm[3]); L += dpp<XOR1>(L); L += dpp<XOR2>(L);
    const float w = e * __builtin_amdgcn_rcpf(L);
    float w0 = dpp<0x00>(w), w1 = dpp<0x55>(w), w2 = dpp<0xAA>(w), o = dpp<0xFF>(w) * oe;
    asm volatile("" : "+v"(w0), "+v"(w1), "+v"(w2), "+v"(o));
    if (!ALL((g[0][0] == tag) & (g[1][0] == tag) & (g[2][0] == tag))) {
      _Pragma("unroll 1") for (unsigned spin = 0; spin < (1u << 22); ++spin) {
        CB();
        ldv();
        if (ALL((g[0][0] == tag) & (g[1][0] == tag) & (g[2][0] == tag))) break;
      }
    }
    o = fmaf(w2, __uint_as_float(g[2][1]), fmaf(w1, __uint_as_float(g[1][1]), fmaf(w0, __uint_as_float(g[0][1]), o)));
    __builtin_amdgcn_raw_buffer_store_b16(__builtin_bit_cast(unsigned short, (__bf16)o), ro, oo, 0, 16);
    __builtin_amdgcn_raw_buffer_store_b32(tag, rw, eo, gb, 16);
  }
}

extern "C" void kernel_launch(void* const* d_in, const int* in_sizes, int n_in, void* d_out, int out_size, void* d_ws,
                              size_t ws_size, hipStream_t stream) {
  gqa_decode<<<NGRP * 4, NW * 64, 0, stream>>>((const unsigned char*)d_in[0], (const unsigned char*)d_in[1], (const unsigned char*)d_in[2], (unsigned char*)d_ws, (unsigned short*)d_out);
}
```

```cpp
#include <hip/hip_runtime.h>
#include <hip/hip_bf16.h>
#include <cstdint>

#define NSPLIT 4
#define NW 16
#define VPRE 8
#define KV_NT 1

constexpr int Bn = 4, HQ = 32, HKV = 16, SK = 4096, D = 128, G = 2;
constexpr int NGRP = Bn * HKV;
constexpr int KPW = SK / (NSPLIT * NW);
constexpr int NLD = KPW / 4;
constexpr int NG = NLD / 8;
static_assert(NLD % 8 == 0 && VPRE <= NLD, "");
constexpr float C2 = 0.088388347648318440f * 1.4426950408889634f;

using u32x4 = __attribute__((ext_vector_type(4))) unsigned;
using f32x4 = __attribute__((ext_vector_type(4))) float;
using bf16x2 = __attribute__((ext_vector_type(2))) __bf16;
using bf16x8 = __attribute__((ext_vector_type(8))) __bf16;

__device__ __forceinline__ float dot2(unsigned k, unsigned q, float acc) {
  return __builtin_amdgcn_fdot2_f32_bf16(__builtin_bit_cast(bf16x2, k), __builtin_bit_cast(bf16x2, q), acc, false);
}
template <int CTRL> __device__ __forceinline__ float dpp(float x) {
  return __builtin_bit_cast(float, __builtin_amdgcn_mov_dpp(__builtin_bit_cast(int, x), CTRL, 0xf, 0xf, true));
}
template <int CTRL> __device__ __forceinline__ unsigned dppu(unsigned x) {
  return (unsigned)__builtin_amdgcn_mov_dpp((int)x, CTRL, 0xf, 0xf, true);
}
constexpr int XOR1 = 0xB1, XOR2 = 0x4E, XOR7 = 0x141, XOR8 = 0x128, ROR4 = 0x124, BCAST = 0x150;
__device__ __forceinline__ float xrow_max(float x) {
  auto s = __builtin_amdgcn_permlane16_swap(__float_as_uint(x), __float_as_uint(x), false, false);
  x = fmaxf(__uint_as_float(s[0]), __uint_as_float(s[1]));
  auto t = __builtin_amdgcn_permlane32_swap(__float_as_uint(x), __float_as_uint(x), false, false);
  return fmaxf(__uint_as_float(t[0]), __uint_as_float(t[1]));
}
__device__ __forceinline__ float xrow_sum(float x) {
  auto s = __builtin_amdgcn_permlane16_swap(__float_as_uint(x), __float_as_uint(x), false, false);
  x = __uint_as_float(s[0]) + __uint_as_float(s[1]);
  auto t = __builtin_amdgcn_permlane32_swap(__float_as_uint(x), __float_as_uint(x), false, false);
  return __uint_as_float(t[0]) + __uint_as_float(t[1]);
}
__device__ __forceinline__ unsigned cvtpk(float lo, float hi) {
  bf16x2 r = {(__bf16)lo, (__bf16)hi};
  return __builtin_bit_cast(unsigned, r);
}
__device__ __forceinline__ u32x4 ldkv(const u32x4* p) {
  return __builtin_nontemporal_load(p);
}

#define ST_DECL
#define ST(i)
#define ST_OUT(ws, rec)
constexpr size_t WS_PO = 0, WS_ML = (size_t)NGRP * NSPLIT * 256 * 4;

template <int T> __device__ __forceinline__ void pv_step(f32x4& acc, const u32x4& v, unsigned pk, const u32x4& sel) {
  const unsigned z = dppu<BCAST + 2 * T>(pk);
  u32x4 b;
#pragma unroll
  for (int w = 0; w < 4; ++w) b[w] = __builtin_amdgcn_perm(z, z, sel[w]);
  acc = __builtin_amdgcn_mfma_f32_16x16x32_bf16(__builtin_bit_cast(bf16x8, v), __builtin_bit_cast(bf16x8, b), acc, 0, 0, 0);
}

__global__ void __launch_bounds__(NW * 64) attn_split(const u32x4* __restrict__ q, const u32x4* __restrict__ k,
                                                     const u32x4* __restrict__ v, float* __restrict__ ws) {
  __shared__ __attribute__((aligned(16))) float lds[NW * 256 + NW * 4];
  ST_DECL ST(0);
  const int tid = threadIdx.x, lane = tid & 63, wave = __builtin_amdgcn_readfirstlane(tid >> 6);
  const int c = lane & 15;
  const int grp = blockIdx.x / NSPLIT, split = blockIdx.x % NSPLIT;
  const size_t base = ((size_t)grp * SK + (size_t)(split * NW + wave) * KPW) * 16 + lane;
  const u32x4* kp = k + base;
  const u32x4* vp = v + base;
  const u32x4 q0 = q[(size_t)grp * 32 + c], q1 = q[(size_t)grp * 32 + 16 + c];
  u32x4 K[NLD], V[NLD];
#pragma unroll
  for (int t = 0; t < NLD; ++t) K[t] = ldkv(kp + t * 64);
#pragma unroll
  for (int t = 0; t < VPRE; ++t) V[t] = ldkv(vp + t * 64);
  __builtin_amdgcn_sched_barrier(0);
  ST(1);
  float s[NG];
#pragma unroll
  for (int g = 0; g < NG; ++g) {
    float x[16];
#pragma unroll
    for (int t = 0; t < 8; ++t) {
      float d0 = 0.f, d1 = 0.f;
#pragma unroll
      for (int w = 0; w < 4; ++w) { d0 = dot2(K[g * 8 + t][w], q0[w], d0); d1 = dot2(K[g * 8 + t][w], q1[w], d1); }
      x[2 * t] = d0; x[2 * t + 1] = d1;
    }
    __builtin_amdgcn_sched_barrier(0);
    if (g == 0) ST(2);
    if (g == NG - 1) ST(3);
    constexpr int PER = (NLD - VPRE + NG - 1) / NG;
#pragma unroll
    for (int t = 0; t < PER; ++t) { const int i = VPRE + g * PER + t; if (i < NLD) V[i] = ldkv(vp + i * 64); }
    __builtin_amdgcn_sched_barrier(0);
#define TR_STEP(HALF, CTRL, BIT) _Pragma("unroll") for (int h = 0; h < (HALF); ++h) { \
      const float lo_ = x[h], hi_ = x[h + (HALF)]; const float t1_ = lo_ + dpp<CTRL>(lo_), t2_ = hi_ + dpp<CTRL>(hi_); \
      x[h] = (c & (BIT)) ? t2_ : t1_; }
    TR_STEP(8, XOR8, 8) TR_STEP(4, XOR7, 4) TR_STEP(2, XOR2, 2) TR_STEP(1, XOR1, 1)
#undef TR_STEP
    s[g] = x[0] * C2;
  }
  float m = s[0];
#pragma unroll
  for (int g = 1; g < NG; ++g) m = fmaxf(m, s[g]);
  m = fmaxf(m, dpp<XOR2>(m)); m = fmaxf(m, dpp<ROR4>(m)); m = fmaxf(m, dpp<XOR8>(m)); m = xrow_max(m);
  float l = 0.f;
  unsigned pk[NG];
#pragma unroll
  for (int g = 0; g < NG; ++g) {
    const float p = __builtin_amdgcn_exp2f(s[g] - m);
    l += p;
    pk[g] = cvtpk(p, dpp<XOR1>(p));
  }
  l += dpp<XOR2>(l); l += dpp<ROR4>(l); l += dpp<XOR8>(l); l = xrow_sum(l);
  if (lane < 2) { lds[NW * 256 + wave * 4 + lane] = m; lds[NW * 256 + wave * 4 + 2 + lane] = l; }
  u32x4 sel;
  {
    const int h = c >> 3, j = c & 7;
    const unsigned srcb = h ? 0x0302u : 0x0100u;
    const unsigned pat = (j & 1) ? ((srcb << 16) | 0x0c0cu) : (0x0c0c0000u | srcb);
#pragma unroll
    for (int w = 0; w < 4; ++w) sel[w] = ((j >> 1) == w) ? pat : 0x0c0c0c0cu;
  }
  f32x4 acc = {0.f, 0.f, 0.f, 0.f};
#pragma unroll
  for (int g = 0; g < NG; ++g) {
    pv_step<0>(acc, V[g * 8 + 0], pk[g], sel); pv_step<1>(acc, V[g * 8 + 1], pk[g], sel);
    pv_step<2>(acc, V[g * 8 + 2], pk[g], sel); pv_step<3>(acc, V[g * 8 + 3], pk[g], sel);
    pv_step<4>(acc, V[g * 8 + 4], pk[g], sel); pv_step<5>(acc, V[g * 8 + 5], pk[g], sel);
    pv_step<6>(acc, V[g * 8 + 6], pk[g], sel); pv_step<7>(acc, V[g * 8 + 7], pk[g], sel);
  }
  *reinterpret_cast<f32x4*>(&lds[wave * 256 + lane * 4]) = acc;
  ST(4);
  __syncthreads();
  ST(5);
  if (tid < 256) {
    const int ln = tid >> 2, h = (ln >> 3) & 1;
    float M = -INFINITY;
#pragma unroll
    for (int w = 0; w < NW; ++w) M = fmaxf(M, lds[NW * 256 + w * 4 + h]);
    float L = 0.f, o = 0.f;
#pragma unroll
    for (int w = 0; w < NW; ++w) {
      const float sc = __builtin_amdgcn_exp2f(lds[NW * 256 + w * 4 + h] - M);
      L = fmaf(sc, lds[NW * 256 + w * 4 + 2 + h], L);
      o = fmaf(sc, lds[w * 256 + tid], o);
    }
    float* po = ws + WS_PO / 4 + (size_t)blockIdx.x * 256;
    po[tid] = o;
    if ((tid & ~32) == 0) { float* ml = ws + WS_ML / 4 + (size_t)blockIdx.x * 4; ml[h] = M; ml[2 + h] = L; }
  }
  ST(6);
  ST_OUT(ws, blockIdx.x * NW + wave);
}

__global__ void __launch_bounds__(256) attn_combine(const float* __restrict__ ws, __hip_bfloat16* __restrict__ out) {
  ST_DECL ST(0);
  const int grp = blockIdx.x, tid = threadIdx.x;
  const int ln = tid >> 2, reg = tid & 3, h = (ln >> 3) & 1, d = 8 * (4 * (ln >> 4) + reg) + (ln & 7);
  const float* po = ws + WS_PO / 4 + (size_t)grp * NSPLIT * 256;
  const float* ml = ws + WS_ML / 4 + (size_t)grp * NSPLIT * 4;
  float M = -INFINITY;
#pragma unroll
  for (int s = 0; s < NSPLIT; ++s) M = fmaxf(M, ml[s * 4 + h]);
  float L = 0.f, o = 0.f;
#pragma unroll
  for (int s = 0; s < NSPLIT; ++s) {
    const float sc = __builtin_amdgcn_exp2f(ml[s * 4 + h] - M);
    L = fmaf(sc, ml[s * 4 + 2 + h], L);
    o = fmaf(sc, po[s * 256 + tid], o);
  }
  out[(size_t)(grp * 2 + h) * D + d] = __float2bfloat16(o / L);
  ST(7);
  ST_OUT(ws, 8192 + blockIdx.x * 4 + (tid >> 6));
}

extern "C" void kernel_launch(void* const* d_in, const int* in_sizes, int n_in, void* d_out, int out_size, void* d_ws,
                              size_t ws_size, hipStream_t stream) {
  attn_split<<<NGRP * NSPLIT, NW * 64, 0, stream>>>((const u32x4*)d_in[0], (const u32x4*)d_in[1], (const u32x4*)d_in[2], (float*)d_ws);
  attn_combine<<<NGRP, 256, 0, stream>>>((const float*)d_ws, (__hip_bfloat16*)d_out);
}
```

```cpp
#include <hip/hip_runtime.h>
#include <hip/hip_bf16.h>
#include <cstdint>
#include <utility>

#define WIN 10
#define WMAX WIN
#define G0 128
#define GD 1
#define W0 WIN
#define NB (WMAX > W0 ? WMAX : W0)
constexpr int nis(int i) {
  int w = i < G0 ? WIN : WIN + (i - G0) / GD + 1;
  if (w > WMAX) w = WMAX;
  int n = i + 1 + w;
  if (n < W0) n = W0;
  return n > 128 ? 128 : n;
}
constexpr int iall() { int i = 0; while (nis(i) < 128) ++i; return i; }
#define AUXS 3
#define PW 96
#define MLW sp
#define BAR1 104

#define PD 1340
#define PO -250
#define TPOLL 800
#define SWT 15
#define NB0 2
#define BAR0 0
#define TBL 1
#define TPS 2
#define PG 2
constexpr int due(int i) { return (PO * 10 + i * PD) / 100; }

static_assert(due(NB0) >= 0);
constexpr int quad(int i) { return ((i ^ (i >> 3)) & 1) | ((i >> 3) & 2); }
constexpr int NW = 4, NGRP = 64;
constexpr float C2 = 0.088388347648318440f * 1.4426950408889634f;

using u32x4 = __attribute__((ext_vector_type(4))) unsigned;
using u32x2 = __attribute__((ext_vector_type(2))) unsigned;
using f32x2 = __attribute__((ext_vector_type(2))) float;
using f32x4 = __attribute__((ext_vector_type(4))) float;
using bf16x2 = __attribute__((ext_vector_type(2))) __bf16;
using bf16x8 = __attribute__((ext_vector_type(8))) __bf16;

__device__ __forceinline__ float dot2(unsigned k, unsigned q, float acc) {
  return __builtin_amdgcn_fdot2_f32_bf16(__builtin_bit_cast(bf16x2, k), __builtin_bit_cast(bf16x2, q), acc, false);
}
template <int CTRL> __device__ __forceinline__ float dpp(float x) {
  return __builtin_bit_cast(float, __builtin_amdgcn_mov_dpp(__builtin_bit_cast(int, x), CTRL, 0xf, 0xf, true));
}
template <int CTRL> __device__ __forceinline__ unsigned dppu(unsigned x) {
  return (unsigned)__builtin_amdgcn_mov_dpp((int)x, CTRL, 0xf, 0xf, true);
}
constexpr int XOR1 = 0xB1, XOR2 = 0x4E, XOR7 = 0x141, XOR8 = 0x128, ROR4 = 0x124, BCAST = 0x150, EVEN = 0xA0, ODD = 0xF5;
__device__ __forceinline__ float xrow_max(float x) {
  auto s = __builtin_amdgcn_permlane16_swap(__float_as_uint(x), __float_as_uint(x), false, false);
  x = fmaxf(__uint_as_float(s[0]), __uint_as_float(s[1]));
  auto t = __builtin_amdgcn_permlane32_swap(__float_as_uint(x), __float_as_uint(x), false, false);
  return fmaxf(__uint_as_float(t[0]), __uint_as_float(t[1]));
}
__device__ __forceinline__ float xrow_sum(float x) {
  auto s = __builtin_amdgcn_permlane16_swap(__float_as_uint(x), __float_as_uint(x), false, false);
  x = __uint_as_float(s[0]) + __uint_as_float(s[1]);
  auto t = __builtin_amdgcn_permlane32_swap(__float_as_uint(x), __float_as_uint(x), false, false);
  return __uint_as_float(t[0]) + __uint_as_float(t[1]);
}
__device__ __forceinline__ unsigned cvtpk(float lo, float hi) {
  bf16x2 r = {(__bf16)lo, (__bf16)hi};
  return __builtin_bit_cast(unsigned, r);
}
__device__ __forceinline__ float ex2(float x) { return __builtin_amdgcn_exp2f(x); }
__device__ __forceinline__ void wg_barrier() {
  asm volatile("s_waitcnt lgkmcnt(0)" ::: "memory");
  __builtin_amdgcn_s_barrier();
  asm volatile("" ::: "memory");
}
template <class F, int... I> __device__ __forceinline__ void sfor_(F&& f, std::integer_sequence<int, I...>) { (f(std::integral_constant<int, I>{}), ...); }
template <int N, class F> __device__ __forceinline__ void sfor(F&& f) { sfor_(f, std::make_integer_sequence<int, N>{}); }
#define SB() __builtin_amdgcn_sched_barrier(0)
#define CB() asm volatile("" ::: "memory")
#define UNTIL(t) asm volatile("1:\n\ts_memrealtime s[60:61]\n\ts_waitcnt lgkmcnt(0)\n\ts_sub_u32 s60, s60, %0\n\ts_cmp_lt_u32 s60, %1\n\ts_cbranch_scc1 1b" \
                              :: "s"(tb), "n"(t) : "s60", "s61", "scc")
#define ALL(x) (__builtin_amdgcn_ballot_w64(x) == ~0ull)

constexpr unsigned GRP_BYTES = 8192, WS_ML = 6144, WS_EP = 6272, OOB = 0x40000000u;

template <int T> __device__ __forceinline__ u32x4 frag(unsigned pk, const u32x4& sel) {
  const unsigned z = dppu<BCAST + 2 * T>(pk);
  u32x4 b;
#pragma unroll
  for (int w = 0; w < 4; ++w) b[w] = __builtin_amdgcn_perm(z, z, sel[w]);
  return b;
}
__device__ __forceinline__ void mma(f32x4& acc, const u32x4& a, const u32x4& v) {
  acc = __builtin_amdgcn_mfma_f32_16x16x32_bf16(__builtin_bit_cast(bf16x8, a), __builtin_bit_cast(bf16x8, v), acc, 0, 0, 0);
}

__global__ void __launch_bounds__(NW * 64, 2) gqa_decode(const unsigned char* __restrict__ q, const unsigned char* __restrict__ k,
                                                        const unsigned char* __restrict__ v, unsigned char* ws,
                                                        unsigned short* out) {
  __shared__ __attribute__((aligned(16))) float lds[NW * 256 + 20];
  const int tid = threadIdx.x, lane = tid & 63, wave = __builtin_amdgcn_readfirstlane(tid >> 6);
  const int c = lane & 15;
  const int grp = (blockIdx.x & 7) * 8 + (blockIdx.x >> 5), sp = (blockIdx.x >> 3) & 3;
  const unsigned vb = lane * 16 + wave * 4096 + grp * 1048576, ct = (grp & SWT) << 14;
  unsigned vo[4];
#pragma unroll
  for (int x = 0; x < 4; ++x) vo[x] = vb + ((x ^ sp) << 10);
  unsigned qo = c * 16 + grp * 512;
  asm volatile("" : "+v"(vo[0]), "+v"(vo[1]), "+v"(vo[2]), "+v"(vo[3]), "+v"(qo));
  SB();
  const auto rk = __builtin_amdgcn_make_buffer_rsrc((void*)k, 0, 64u << 20, 0x00020000);
  u32x4 R[NB], q0, q1;
  const int h = lane >> 5;
  unsigned oo, g0, gb, tag, tb;
  unsigned short o0;
  unsigned cp, tp;
  __amdgpu_buffer_rsrc_t rv, rw, ro;
  auto issue = [&](auto ic) {
    constexpr int I = decltype(ic)::value;
    if constexpr (I >= NB0 && I < 128 && I % PG == 0) {
      UNTIL(due(I));
      SB();
    }
    if constexpr (I < 64) R[I % NB] = __builtin_amdgcn_raw_buffer_load_b128(rk, vo[quad(I)], (I * 16384) ^ ct, AUXS);
    else if constexpr (I < 128) R[I % NB] = __builtin_amdgcn_raw_buffer_load_b128(rv, vo[quad(I)], ((I - 64) * 16384) ^ ct, AUXS);
    SB();
    if constexpr (I == NB0 - 1) {
      if constexpr (BAR0 == 1) __builtin_amdgcn_s_barrier();
      unsigned long long t64;
      if constexpr (!TBL) t64 = __builtin_amdgcn_s_memrealtime();
      SB();
      const unsigned char* tpp = sp == 0 ? v + grp * 1048576 : sp == 1 ? ws + grp * GRP_BYTES : (const unsigned char*)out + grp * 512;
      tp = __builtin_amdgcn_raw_buffer_load_b32(__builtin_amdgcn_make_buffer_rsrc((void*)tpp, 0, (wave | (sp >> TPS)) ? 0 : 4, 0x00020000), 0, 0, 0);
      const auto rq = __builtin_amdgcn_make_buffer_rsrc((void*)q, 0, 32768, 0x00020000);
      q0 = __builtin_amdgcn_raw_buffer_load_b128(rq, qo, 0, 0);
      q1 = __builtin_amdgcn_raw_buffer_load_b128(rq, qo + 256, 0, 0);
      unsigned long long pc;
      unsigned len;
      asm volatile("s_getpc_b64 %0\n\ts_mov_b32 %1, .Lgqa_end-." : "=s"(pc), "=s"(len));
      cp = __builtin_amdgcn_raw_buffer_load_b32(__builtin_amdgcn_make_buffer_rsrc((void*)pc, 0, len, 0x00020000), ((blockIdx.x >> 3) * 4 + wave) * 128 + 256, 0, 0);
      rv = __builtin_amdgcn_make_buffer_rsrc((void*)v, 0, 64u << 20, 0x00020000);
      rw = __builtin_amdgcn_make_buffer_rsrc(ws, 0, NGRP * GRP_BYTES, 0x00020000);
      ro = __builtin_amdgcn_make_buffer_rsrc(out, 0, 32768, 0x00020000);
      gb = grp * GRP_BYTES;
      SB();
      if constexpr (BAR0 == 2) __builtin_amdgcn_s_barrier();
      if constexpr (TBL) t64 = __builtin_amdgcn_s_memrealtime();
      SB();
      asm volatile("" : "+s"(t64));
      tb = (unsigned)t64;
      oo = (grp * 256 + h * 128 + sp * 32 + (lane & 31)) * 2;
    }
    if constexpr (I == PW) {
      CB();
      const unsigned skip = wave == sp ? 0u : OOB;
      g0 = __builtin_amdgcn_raw_buffer_load_b32(rw, (lane < 12 ? sp * 1536 + lane * 128 : lane < 14 ? WS_ML + (lane - 12) * 128 : OOB) | skip, gb, 16);
      o0 = __builtin_amdgcn_raw_buffer_load_b16(ro, oo | skip, 0, 16);
      CB();
      SB();
    }
  };
  sfor<nis(-1)>(issue);
  auto step = [&](auto ic) { constexpr int I = decltype(ic)::value; sfor<nis(I) - nis(I - 1)>([&](auto dc) { issue(std::integral_constant<int, nis(I - 1) + decltype(dc)::value>{}); }); };
  float s[8];
  sfor<8>([&](auto gc) {
    constexpr int G = decltype(gc)::value;
    float x[16];
    sfor<8>([&](auto tc) {
      constexpr int T = decltype(tc)::value, I = G * 8 + T;
      float d0 = 0.f, d1 = 0.f;
#pragma unroll
      for (int w = 0; w < 4; ++w) { d0 = dot2(R[I % NB][w], q0[w], d0); d1 = dot2(R[I % NB][w], q1[w], d1); }
      x[2 * T] = d0; x[2 * T + 1] = d1;
      SB();
      step(std::integral_constant<int, I>{});
    });
#define TR_STEP(HALF, CTRL, BIT) _Pragma("unroll") for (int h = 0; h < (HALF); ++h) { \
      const float lo_ = x[h], hi_ = x[h + (HALF)]; const float t1_ = lo_ + dpp<CTRL>(lo_), t2_ = hi_ + dpp<CTRL>(hi_); \
      x[h] = (c & (BIT)) ? t2_ : t1_; }
    TR_STEP(8, XOR8, 8) TR_STEP(4, XOR7, 4) TR_STEP(2, XOR2, 2) TR_STEP(1, XOR1, 1)
#undef TR_STEP
    s[G] = x[0] * C2;
    SB();
  });
  asm volatile("" :: "v"(cp), "v"(tp));
  float m = s[0];
#pragma unroll
  for (int g = 1; g < 8; ++g) m = fmaxf(m, s[g]);
  m = fmaxf(m, dpp<XOR2>(m)); m = fmaxf(m, dpp<ROR4>(m)); m = fmaxf(m, dpp<XOR8>(m)); m = xrow_max(m);
  float l = 0.f;
  unsigned pk[8];
#pragma unroll
  for (int g = 0; g < 8; ++g) {
    const float p = ex2(s[g] - m);
    l += p;
    pk[g] = cvtpk(p, dpp<XOR1>(p));
  }
  u32x4 sel;
  {
    const int h = c >> 3, j = c & 7;
    const unsigned srcb = h ? 0x0302u : 0x0100u;
    const unsigned pat = (j & 1) ? ((srcb << 16) | 0x0c0cu) : (0x0c0c0000u | srcb);
#pragma unroll
    for (int w = 0; w < 4; ++w) sel[w] = ((j >> 1) == w) ? pat : 0x0c0c0c0cu;
  }
  SB();
  f32x4 acc = {0.f, 0.f, 0.f, 0.f};
  float* const lml = lds + NW * 256;
  float ew;
  unsigned lw, lr, xo, mo, eo;
  u32x4 gm;
  sfor<64>([&](auto jc) {
    constexpr int J = decltype(jc)::value, I = 64 + J;
    if constexpr (I == BAR1) {
      wg_barrier();
      const f32x4 a = *reinterpret_cast<const f32x4*>(lml + h * 8), b = *reinterpret_cast<const f32x4*>(lml + h * 8 + 4);
      const float mw = lml[h * 8 + wave];
      tag = __float_as_uint(lml[16]) * 2654435761u + 0x9E3779B9u;
      const float M = fmaxf(fmaxf(a[0], a[1]), fmaxf(a[2], a[3]));
      const float L = fmaf(ex2(a[3] - M), b[3], fmaf(ex2(a[2] - M), b[2], fmaf(ex2(a[1] - M), b[1], ex2(a[0] - M) * b[0])));
      ew = ex2(mw - M);
      __builtin_amdgcn_raw_buffer_store_b128(u32x4{tag, __float_as_uint(M), tag, __float_as_uint(L)}, rw,
                                             ((lane & 31) || wave != MLW) ? OOB : WS_ML + sp * 32 + h * 16, gb, 16);
      lw = wave * 1024 + lane * 16;
      if (wave != sp) {
        lr = (((lane & 16) * 2 + (lane & 2) * 8 + wave * 4 + ((lane >> 2) & 3)) * 4 + (lane & 1) * 2) * 4;
        xo = lane < 32 ? wave * 1536 + ((sp - wave - 1) & 3) * 512 + lane * 16 : OOB;
      } else {
        lr = ((h * 32 + (lane & 4) * 4 + wave * 4 + ((lane >> 3) & 3)) * 4 + (lane & 3)) * 4;
        xo = sp * 1536 + lane * 8;
      }
      mo = wave == sp ? WS_ML + ((sp + 1 + lane) & 3) * 32 + h * 16 : OOB;
      eo = (lane | sp) ? OOB : WS_EP;
      asm volatile("" : "+v"(ew), "+v"(tag), "+v"(lw), "+v"(lr), "+v"(xo), "+v"(mo), "+v"(eo));
      SB();
    }
    mma(acc, frag<J & 7>(pk[J >> 3], sel), R[I % NB]);
    SB();
    step(std::integral_constant<int, I>{});
    if constexpr (I == iall()) { CB(); gm = __builtin_amdgcn_raw_buffer_load_b128(rw, mo, gb, 16); CB(); }
    if constexpr (I == 64 + WIN) {
      l += dpp<XOR2>(l); l += dpp<ROR4>(l); l += dpp<XOR8>(l); l = xrow_sum(l);
      if (lane < 2) { lml[lane * 8 + wave] = m; lml[lane * 8 + 4 + wave] = l; }
    }
    if constexpr (I == (PW + 1 > 64 ? PW + 1 : 64)) {
      asm volatile("" :: "v"(o0));
      lml[(wave == sp && lane == 13) ? 16 : 17] = __uint_as_float(g0);
    }
    SB();
  });
  *reinterpret_cast<f32x4*>(reinterpret_cast<char*>(lds) + lw) = acc * ew;
  wg_barrier();
  const float* src = reinterpret_cast<const float*>(reinterpret_cast<const char*>(lds) + lr);
  if (wave != sp) {
    const f32x2 o = (*(const f32x2*)src + *(const f32x2*)(src + 256)) + (*(const f32x2*)(src + 512) + *(const f32x2*)(src + 768));
    __builtin_amdgcn_raw_buffer_store_b128(u32x4{tag, __float_as_uint(o[0]), tag, __float_as_uint(o[1])}, rw, xo, gb, 16);
  } else {
    u32x2 g[3];
    auto ldv = [&]() {
#pragma unroll
      for (int j = 0; j < 3; ++j) g[j] = __builtin_amdgcn_raw_buffer_load_b64(rw, xo + j * 512, gb, 16);
    };
    const float oe = (src[0] + src[256]) + (src[512] + src[768]);
    if (!ALL((gm[0] == tag) & (gm[2] == tag))) {
      _Pragma("unroll 1") for (unsigned spin = 0; spin < (1u << 22); ++spin) {
        CB();
        gm = __builtin_amdgcn_raw_buffer_load_b128(rw, mo, gb, 16);
        if (ALL((gm[0] == tag) & (gm[2] == tag))) break;
      }
    }
    const float Mj = __uint_as_float(gm[1]);
    float M = fmaxf(Mj, dpp<XOR1>(Mj)); M = fmaxf(M, dpp<XOR2>(M));
    const float e = ex2(Mj - M);
    float L = e * __uint_as_float(gm[3]); L += dpp<XOR1>(L); L += dpp<XOR2>(L);
    const float w = e * __builtin_amdgcn_rcpf(L);
    float w0 = dpp<0x00>(w), w1 = dpp<0x55>(w), w2 = dpp<0xAA>(w), o = dpp<0xFF>(w) * oe;
    asm volatile("" : "+v"(w0), "+v"(w1), "+v"(w2), "+v"(o));
    SB();
    UNTIL(due(128) + TPOLL / 10);
    CB();
    ldv();
    if (!ALL((g[0][0] == tag) & (g[1][0] == tag) & (g[2][0] == tag))) {
      _Pragma("unroll 1") for (unsigned spin = 0; spin < (1u << 22); ++spin) {
        CB();
        ldv();
        if (ALL((g[0][0] == tag) & (g[1][0] == tag) & (g[2][0] == tag))) break;
      }
    }
    o = fmaf(w2, __uint_as_float(g[2][1]), fmaf(w1, __uint_as_float(g[1][1]), fmaf(w0, __uint_as_float(g[0][1]), o)));
    __builtin_amdgcn_raw_buffer_store_b16(__builtin_bit_cast(unsigned short, (__bf16)o), ro, oo, 0, 16);
    __builtin_amdgcn_raw_buffer_store_b32(tag, rw, eo, gb, 16);
    SB();
    asm volatile(".Lgqa_end:");
  }
}

extern "C" void kernel_launch(void* const* d_in, const int* in_sizes, int n_in, void* d_out, int out_size, void* d_ws,
                              size_t ws_size, hipStream_t stream) {
  gqa_decode<<<NGRP * 4, NW * 64, 0, stream>>>((const unsigned char*)d_in[0], (const unsigned char*)d_in[1], (const unsigned char*)d_in[2], (unsigned char*)d_ws, (unsigned short*)d_out);
}
```

```cpp
#include <hip/hip_runtime.h>
#include <hip/hip_bf16.h>
#include <cstdint>
#include <utility>

constexpr int WIN = 10;
constexpr int PD = 1345;
constexpr int PO = -200;
constexpr int TPOLL = 900;
constexpr int PW2 = 51, PW = 97, BAR1 = 104;
constexpr int nis(int i) {
  const int n = (i + 1 + WIN) & ~1;
  return n > 128 ? 128 : n;
}
constexpr int iall() { int i = 0; while (nis(i) < 128) ++i; return i; }
constexpr int due(int i) { return (PO * 10 + i * PD) / 100; }
constexpr int quad(int i) { return ((i ^ (i >> 3)) & 1) | ((i >> 3) & 2); }
constexpr int NW = 4, NGRP = 64;
constexpr float C2 = 0.088388347648318440f * 1.4426950408889634f;

using u32x4 = __attribute__((ext_vector_type(4))) unsigned;
using u32x2 = __attribute__((ext_vector_type(2))) unsigned;
using f32x2 = __attribute__((ext_vector_type(2))) float;
using f32x4 = __attribute__((ext_vector_type(4))) float;
using bf16x2 = __attribute__((ext_vector_type(2))) __bf16;
using bf16x8 = __attribute__((ext_vector_type(8))) __bf16;

__device__ __forceinline__ float dot2(unsigned k, unsigned q, float acc) {
  return __builtin_amdgcn_fdot2_f32_bf16(__builtin_bit_cast(bf16x2, k), __builtin_bit_cast(bf16x2, q), acc, false);
}
template <int CTRL> __device__ __forceinline__ float dpp(float x) {
  return __builtin_bit_cast(float, __builtin_amdgcn_mov_dpp(__builtin_bit_cast(int, x), CTRL, 0xf, 0xf, true));
}
template <int CTRL> __device__ __forceinline__ unsigned dppu(unsigned x) {
  return (unsigned)__builtin_amdgcn_mov_dpp((int)x, CTRL, 0xf, 0xf, true);
}
constexpr int XOR1 = 0xB1, XOR2 = 0x4E, XOR7 = 0x141, XOR8 = 0x128, ROR4 = 0x124, BCAST = 0x150;
__device__ __forceinline__ float xrow_max(float x) {
  auto s = __builtin_amdgcn_permlane16_swap(__float_as_uint(x), __float_as_uint(x), false, false);
  x = fmaxf(__uint_as_float(s[0]), __uint_as_float(s[1]));
  auto t = __builtin_amdgcn_permlane32_swap(__float_as_uint(x), __float_as_uint(x), false, false);
  return fmaxf(__uint_as_float(t[0]), __uint_as_float(t[1]));
}
__device__ __forceinline__ float xrow_sum(float x) {
  auto s = __builtin_amdgcn_permlane16_swap(__float_as_uint(x), __float_as_uint(x), false, false);
  x = __uint_as_float(s[0]) + __uint_as_float(s[1]);
  auto t = __builtin_amdgcn_permlane32_swap(__float_as_uint(x), __float_as_uint(x), false, false);
  return __uint_as_float(t[0]) + __uint_as_float(t[1]);
}
__device__ __forceinline__ unsigned cvtpk(float lo, float hi) {
  bf16x2 r = {(__bf16)lo, (__bf16)hi};
  return __builtin_bit_cast(unsigned, r);
}
__device__ __forceinline__ float ex2(float x) { return __builtin_amdgcn_exp2f(x); }
__device__ __forceinline__ void wg_barrier() {
  asm volatile("s_waitcnt lgkmcnt(0)" ::: "memory");
  __builtin_amdgcn_s_barrier();
  asm volatile("" ::: "memory");
}
template <class F, int... I> __device__ __forceinline__ void sfor_(F&& f, std::integer_sequence<int, I...>) { (f(std::integral_constant<int, I>{}), ...); }
template <int N, class F> __device__ __forceinline__ void sfor(F&& f) { sfor_(f, std::make_integer_sequence<int, N>{}); }
#define SB() __builtin_amdgcn_sched_barrier(0)
#define CB() asm volatile("" ::: "memory")
#define UNTIL(t) asm volatile("1:\n\ts_memrealtime s[60:61]\n\ts_waitcnt lgkmcnt(0)\n\ts_sub_u32 s60, s60, %0\n\ts_cmp_lt_u32 s60, %1\n\ts_cbranch_scc1 1b" \
                              :: "s"(tb), "n"(t) : "s60", "s61", "scc")
#define ALL(x) (__builtin_amdgcn_ballot_w64(x) == ~0ull)
static_assert(due(2) >= 0);

constexpr unsigned GRP_BYTES = 8192, WS_ML = 6144, WS_EP = 6272, OOB = 0x40000000u;

template <int T> __device__ __forceinline__ u32x4 frag(unsigned pk, const u32x4& sel) {
  const unsigned z = dppu<BCAST + 2 * T>(pk);
  u32x4 b;
#pragma unroll
  for (int w = 0; w < 4; ++w) b[w] = __builtin_amdgcn_perm(z, z, sel[w]);
  return b;
}
__device__ __forceinline__ void mma(f32x4& acc, const u32x4& a, const u32x4& v) {
  acc = __builtin_amdgcn_mfma_f32_16x16x32_bf16(__builtin_bit_cast(bf16x8, a), __builtin_bit_cast(bf16x8, v), acc, 0, 0, 0);
}

__global__ void __launch_bounds__(NW * 64, 2) gqa_decode(const unsigned char* __restrict__ q, const unsigned char* __restrict__ k,
                                                        const unsigned char* __restrict__ v, unsigned char* ws,
                                                        unsigned short* out) {
  __shared__ __attribute__((aligned(16))) float lds[NW * 256 + 20];
  const int tid = threadIdx.x, lane = tid & 63, wave = __builtin_amdgcn_readfirstlane(tid >> 6);
  const int c = lane & 15;
  const int grp = (blockIdx.x & 7) * 8 + (blockIdx.x >> 5), sp = (blockIdx.x >> 3) & 3;
  const unsigned vb = lane * 16 + wave * 4096 + grp * 1048576, ct = (grp & 15) << 14;
  unsigned vo[4];
#pragma unroll
  for (int x = 0; x < 4; ++x) vo[x] = vb + ((x ^ sp) << 10);
  unsigned qo = c * 16 + grp * 512;
  asm volatile("" : "+v"(vo[0]), "+v"(vo[1]), "+v"(vo[2]), "+v"(vo[3]), "+v"(qo));
  SB();
  const auto rk = __builtin_amdgcn_make_buffer_rsrc((void*)k, 0, 64u << 20, 0x00020000);
  u32x4 R[WIN], q0, q1;
  const int h = lane >> 5;
  unsigned oo, g0, gb, tag, tb;
  unsigned short o0;
  unsigned cp, tp, g2;
  __amdgpu_buffer_rsrc_t rv, rw, ro;
  auto issue = [&](auto ic) {
    constexpr int I = decltype(ic)::value;
    if constexpr (I >= 2 && I % 2 == 0) {
      UNTIL(due(I));
      SB();
    }
    if constexpr (I < 64) R[I % WIN] = __builtin_amdgcn_raw_buffer_load_b128(rk, vo[quad(I)], (I * 16384) ^ ct, 3);
    else R[I % WIN] = __builtin_amdgcn_raw_buffer_load_b128(rv, vo[quad(I)], ((I - 64) * 16384) ^ ct, 3);
    SB();
    if constexpr (I == 1) {
      const unsigned char* tpp = sp == 0 ? v + grp * 1048576 : sp == 1 ? ws + grp * GRP_BYTES : (const unsigned char*)out + grp * 512;
      tp = __builtin_amdgcn_raw_buffer_load_b32(__builtin_amdgcn_make_buffer_rsrc((void*)tpp, 0, wave ? 0 : 4, 0x00020000), 0, 0, 0);
      const auto rq = __builtin_amdgcn_make_buffer_rsrc((void*)q, 0, 32768, 0x00020000);
      q0 = __builtin_amdgcn_raw_buffer_load_b128(rq, qo, 0, 0);
      q1 = __builtin_amdgcn_raw_buffer_load_b128(rq, qo + 256, 0, 0);
      unsigned long long pc;
      unsigned len;
      asm volatile("s_getpc_b64 %0\n\ts_mov_b32 %1, .Lgqa_end-." : "=s"(pc), "=s"(len));
      cp = __builtin_amdgcn_raw_buffer_load_b32(__builtin_amdgcn_make_buffer_rsrc((void*)pc, 0, len, 0x00020000), ((blockIdx.x >> 3) * 4 + wave) * 128 + 256, 0, 0);
      rv = __builtin_amdgcn_make_buffer_rsrc((void*)v, 0, 64u << 20, 0x00020000);
      rw = __builtin_amdgcn_make_buffer_rsrc(ws, 0, NGRP * GRP_BYTES, 0x00020000);
      ro = __builtin_amdgcn_make_buffer_rsrc(out, 0, 32768, 0x00020000);
      gb = grp * GRP_BYTES;
      SB();
      unsigned long long t64 = __builtin_amdgcn_s_memrealtime();
      SB();
      asm volatile("" : "+s"(t64));
      tb = (unsigned)t64;
      oo = (grp * 256 + h * 128 + sp * 32 + (lane & 31)) * 2;
    }
    if constexpr (I == PW2) {
      CB();
      g2 = __builtin_amdgcn_raw_buffer_load_b32(rw, lane < 3 ? sp * 1536 + (wave * 3 + lane) * 128 : OOB, gb, 16);
      CB();
      SB();
    }
    if constexpr (I == PW) {
      CB();
      const unsigned skip = wave == sp ? 0u : OOB;
      g0 = __builtin_amdgcn_raw_buffer_load_b32(rw, (lane < 12 ? OOB : lane < 14 ? WS_ML + (lane - 12) * 128 : OOB) | skip, gb, 16);
      o0 = __builtin_amdgcn_raw_buffer_load_b16(ro, oo | skip, 0, 16);
      CB();
      SB();
    }
  };
  sfor<nis(-1)>(issue);
  auto step = [&](auto ic) { constexpr int I = decltype(ic)::value; sfor<nis(I) - nis(I - 1)>([&](auto dc) { issue(std::integral_constant<int, nis(I - 1) + decltype(dc)::value>{}); }); };
  float s[8];
  sfor<8>([&](auto gc) {
    constexpr int G = decltype(gc)::value;
    float x[16];
    sfor<8>([&](auto tc) {
      constexpr int T = decltype(tc)::value, I = G * 8 + T;
      float d0 = 0.f, d1 = 0.f;
#pragma unroll
      for (int w = 0; w < 4; ++w) { d0 = dot2(R[I % WIN][w], q0[w], d0); d1 = dot2(R[I % WIN][w], q1[w], d1); }
      x[2 * T] = d0; x[2 * T + 1] = d1;
      SB();
      step(std::integral_constant<int, I>{});
    });
#define TR_STEP(HALF, CTRL, BIT) _Pragma("unroll") for (int h = 0; h < (HALF); ++h) { \
      const float lo_ = x[h], hi_ = x[h + (HALF)]; const float t1_ = lo_ + dpp<CTRL>(lo_), t2_ = hi_ + dpp<CTRL>(hi_); \
      x[h] = (c & (BIT)) ? t2_ : t1_; }
    TR_STEP(8, XOR8, 8) TR_STEP(4, XOR7, 4) TR_STEP(2, XOR2, 2) TR_STEP(1, XOR1, 1)
#undef TR_STEP
    s[G] = x[0] * C2;
    SB();
  });
  asm volatile("" :: "v"(cp), "v"(tp));
  float m = s[0];
#pragma unroll
  for (int g = 1; g < 8; ++g) m = fmaxf(m, s[g]);
  m = fmaxf(m, dpp<XOR2>(m)); m = fmaxf(m, dpp<ROR4>(m)); m = fmaxf(m, dpp<XOR8>(m)); m = xrow_max(m);
  float l = 0.f;
  unsigned pk[8];
#pragma unroll
  for (int g = 0; g < 8; ++g) {
    const float p = ex2(s[g] - m);
    l += p;
    pk[g] = cvtpk(p, dpp<XOR1>(p));
  }
  u32x4 sel;
  {
    const int h = c >> 3, j = c & 7;
    const unsigned srcb = h ? 0x0302u : 0x0100u;
    const unsigned pat = (j & 1) ? ((srcb << 16) | 0x0c0cu) : (0x0c0c0000u | srcb);
#pragma unroll
    for (int w = 0; w < 4; ++w) sel[w] = ((j >> 1) == w) ? pat : 0x0c0c0c0cu;
  }
  SB();
  f32x4 acc = {0.f, 0.f, 0.f, 0.f};
  float* const lml = lds + NW * 256;
  float ew;
  unsigned lw, lr, xo, mo, eo;
  u32x4 gm;
  sfor<64>([&](auto jc) {
    constexpr int J = decltype(jc)::value, I = 64 + J;
    if constexpr (I == BAR1) {
      wg_barrier();
      const f32x4 a = *reinterpret_cast<const f32x4*>(lml + h * 8), b = *reinterpret_cast<const f32x4*>(lml + h * 8 + 4);
      const float mw = lml[h * 8 + wave];
      tag = __float_as_uint(lml[16]) * 2654435761u + 0x9E3779B9u;
      const float M = fmaxf(fmaxf(a[0], a[1]), fmaxf(a[2], a[3]));
      const float L = fmaf(ex2(a[3] - M), b[3], fmaf(ex2(a[2] - M), b[2], fmaf(ex2(a[1] - M), b[1], ex2(a[0] - M) * b[0])));
      ew = ex2(mw - M);
      __builtin_amdgcn_raw_buffer_store_b128(u32x4{tag, __float_as_uint(M), tag, __float_as_uint(L)}, rw,
                                             ((lane & 31) || wave != sp) ? OOB : WS_ML + sp * 32 + h * 16, gb, 16);
      lw = wave * 1024 + lane * 16;
      if (wave != sp) {
        lr = (((lane & 16) * 2 + (lane & 2) * 8 + wave * 4 + ((lane >> 2) & 3)) * 4 + (lane & 1) * 2) * 4;
        xo = lane < 32 ? wave * 1536 + ((sp - wave - 1) & 3) * 512 + lane * 16 : OOB;
      } else {
        lr = ((h * 32 + (lane & 4) * 4 + wave * 4 + ((lane >> 3) & 3)) * 4 + (lane & 3)) * 4;
        xo = sp * 1536 + lane * 8;
      }
      mo = wave == sp ? WS_ML + ((sp + 1 + lane) & 3) * 32 + h * 16 : OOB;
      eo = (lane | sp) ? OOB : WS_EP;
      asm volatile("" : "+v"(ew), "+v"(tag), "+v"(lw), "+v"(lr), "+v"(xo), "+v"(mo), "+v"(eo));
      SB();
    }
    mma(acc, frag<J & 7>(pk[J >> 3], sel), R[I % WIN]);
    SB();
    step(std::integral_constant<int, I>{});
    if constexpr (I == iall()) { CB(); gm = __builtin_amdgcn_raw_buffer_load_b128(rw, mo, gb, 16); CB(); }
    if constexpr (I == 64 + WIN) {
      l += dpp<XOR2>(l); l += dpp<ROR4>(l); l += dpp<XOR8>(l); l = xrow_sum(l);
      if (lane < 2) { lml[lane * 8 + wave] = m; lml[lane * 8 + 4 + wave] = l; }
    }
    if constexpr (I == PW + 1) {
      asm volatile("" :: "v"(o0));
      lml[(wave == sp && lane == 13) ? 16 : 17] = __uint_as_float(g0);
    }
    SB();
  });
  *reinterpret_cast<f32x4*>(reinterpret_cast<char*>(lds) + lw) = acc * ew;
  asm volatile("" :: "v"(g2));
  wg_barrier();
  const float* src = reinterpret_cast<const float*>(reinterpret_cast<const char*>(lds) + lr);
  if (wave != sp) {
    const f32x2 o = (*(const f32x2*)src + *(const f32x2*)(src + 256)) + (*(const f32x2*)(src + 512) + *(const f32x2*)(src + 768));
    __builtin_amdgcn_raw_buffer_store_b128(u32x4{tag, __float_as_uint(o[0]), tag, __float_as_uint(o[1])}, rw, xo, gb, 16);
  } else {
    u32x2 g[3];
    auto ldv = [&]() {
#pragma unroll
      for (int j = 0; j < 3; ++j) g[j] = __builtin_amdgcn_raw_buffer_load_b64(rw, xo + j * 512, gb, 16);
    };
    const float oe = (src[0] + src[256]) + (src[512] + src[768]);
    if (!ALL((gm[0] == tag) & (gm[2] == tag))) {
      _Pragma("unroll 1") for (unsigned spin = 0; spin < (1u << 22); ++spin) {
        CB();
        gm = __builtin_amdgcn_raw_buffer_load_b128(rw, mo, gb, 16);
        if (ALL((gm[0] == tag) & (gm[2] == tag))) break;
      }
    }
    const float Mj = __uint_as_float(gm[1]);
    float M = fmaxf(Mj, dpp<XOR1>(Mj)); M = fmaxf(M, dpp<XOR2>(M));
    const float e = ex2(Mj - M);
    float L = e * __uint_as_float(gm[3]); L += dpp<XOR1>(L); L += dpp<XOR2>(L);
    const float w = e * __builtin_amdgcn_rcpf(L);
    float w0 = dpp<0x00>(w), w1 = dpp<0x55>(w), w2 = dpp<0xAA>(w), o = dpp<0xFF>(w) * oe;
    asm volatile("" : "+v"(w0), "+v"(w1), "+v"(w2), "+v"(o));
    SB();
    UNTIL(due(128) + TPOLL / 10);
    CB();
    ldv();
    if (!ALL((g[0][0] == tag) & (g[1][0] == tag) & (g[2][0] == tag))) {
      _Pragma("unroll 1") for (unsigned spin = 0; spin < (1u << 22); ++spin) {
        CB();
        ldv();
        if (ALL((g[0][0] == tag) & (g[1][0] == tag) & (g[2][0] == tag))) break;
      }
    }
    o = fmaf(w2, __uint_as_float(g[2][1]), fmaf(w1, __uint_as_float(g[1][1]), fmaf(w0, __uint_as_float(g[0][1]), o)));
    __builtin_amdgcn_raw_buffer_store_b16(__builtin_bit_cast(unsigned short, (__bf16)o), ro, oo, 0, 16);
    __builtin_amdgcn_raw_buffer_store_b32(tag, rw, eo, gb, 16);
    SB();
    asm volatile(".Lgqa_end:");
  }
}

extern "C" void kernel_launch(void* const* d_in, const int* in_sizes, int n_in, void* d_out, int out_size, void* d_ws,
                              size_t ws_size, hipStream_t stream) {
  gqa_decode<<<NGRP * 4, NW * 64, 0, stream>>>((const unsigned char*)d_in[0], (const unsigned char*)d_in[1], (const unsigned char*)d_in[2], (unsigned char*)d_ws, (unsigned short*)d_out);
}
```

```cpp
#include <hip/hip_runtime.h>
#include <hip/hip_bf16.h>
#include <cstdint>
#include <utility>

constexpr int WIN = 10;
constexpr int PD = 1345;
constexpr int PO = -200;
constexpr int TPOLL = 900;
constexpr int PW = 90, BAR1 = 104, PW2 = 115, NPR = 3;
constexpr int nis(int i) {
  const int n = (i + 1 + WIN) & ~1;
  return n > 128 ? 128 : n;
}
constexpr int iall() { int i = 0; while (nis(i) < 128) ++i; return i; }
constexpr int due(int i) { return (PO * 10 + i * PD) / 100; }
constexpr int quad(int i) { return ((i ^ (i >> 3)) & 1) | ((i >> 3) & 2); }
constexpr int NW = 4, NGRP = 64;
constexpr float C2 = 0.088388347648318440f * 1.4426950408889634f;

using u32x4 = __attribute__((ext_vector_type(4))) unsigned;
using u32x2 = __attribute__((ext_vector_type(2))) unsigned;
using f32x2 = __attribute__((ext_vector_type(2))) float;
using f32x4 = __attribute__((ext_vector_type(4))) float;
using bf16x2 = __attribute__((ext_vector_type(2))) __bf16;
using bf16x8 = __attribute__((ext_vector_type(8))) __bf16;

__device__ __forceinline__ float dot2(unsigned k, unsigned q, float acc) {
  return __builtin_amdgcn_fdot2_f32_bf16(__builtin_bit_cast(bf16x2, k), __builtin_bit_cast(bf16x2, q), acc, false);
}
template <int CTRL> __device__ __forceinline__ float dpp(float x) {
  return __builtin_bit_cast(float, __builtin_amdgcn_mov_dpp(__builtin_bit_cast(int, x), CTRL, 0xf, 0xf, true));
}
template <int CTRL> __device__ __forceinline__ unsigned dppu(unsigned x) {
  return (unsigned)__builtin_amdgcn_mov_dpp((int)x, CTRL, 0xf, 0xf, true);
}
constexpr int XOR1 = 0xB1, XOR2 = 0x4E, XOR7 = 0x141, XOR8 = 0x128, ROR4 = 0x124, BCAST = 0x150;
__device__ __forceinline__ float xrow_max(float x) {
  auto s = __builtin_amdgcn_permlane16_swap(__float_as_uint(x), __float_as_uint(x), false, false);
  x = fmaxf(__uint_as_float(s[0]), __uint_as_float(s[1]));
  auto t = __builtin_amdgcn_permlane32_swap(__float_as_uint(x), __float_as_uint(x), false, false);
  return fmaxf(__uint_as_float(t[0]), __uint_as_float(t[1]));
}
__device__ __forceinline__ float xrow_sum(float x) {
  auto s = __builtin_amdgcn_permlane16_swap(__float_as_uint(x), __float_as_uint(x), false, false);
  x = __uint_as_float(s[0]) + __uint_as_float(s[1]);
  auto t = __builtin_amdgcn_permlane32_swap(__float_as_uint(x), __float_as_uint(x), false, false);
  return __uint_as_float(t[0]) + __uint_as_float(t[1]);
}
__device__ __forceinline__ unsigned cvtpk(float lo, float hi) {
  bf16x2 r = {(__bf16)lo, (__bf16)hi};
  return __builtin_bit_cast(unsigned, r);
}
__device__ __forceinline__ float ex2(float x) { return __builtin_amdgcn_exp2f(x); }
__device__ __forceinline__ void wg_barrier() {
  asm volatile("s_waitcnt lgkmcnt(0)" ::: "memory");
  __builtin_amdgcn_s_barrier();
  asm volatile("" ::: "memory");
}
template <class F, int... I> __device__ __forceinline__ void sfor_(F&& f, std::integer_sequence<int, I...>) { (f(std::integral_constant<int, I>{}), ...); }
template <int N, class F> __device__ __forceinline__ void sfor(F&& f) { sfor_(f, std::make_integer_sequence<int, N>{}); }
#define SB() __builtin_amdgcn_sched_barrier(0)
#define CB() asm volatile("" ::: "memory")
#define UNTIL(t) asm volatile("1:\n\ts_memrealtime s[60:61]\n\ts_waitcnt lgkmcnt(0)\n\ts_sub_u32 s60, s60, %0\n\ts_cmp_lt_u32 s60, %1\n\ts_cbranch_scc1 1b" \
                              :: "s"(tb), "n"(t) : "s60", "s61", "scc")
#define ALL(x) (__builtin_amdgcn_ballot_w64(x) == ~0ull)
static_assert(due(2) >= 0 && nis(BAR1 - 1) <= PW2);

constexpr unsigned GRP_BYTES = 16384, WS_ML = 6144, WS_EP = 6272, WS_PR = 6400, OOB = 0x40000000u;
__device__ __forceinline__ unsigned mktag(unsigned epoch) { return epoch * 2654435761u + 0x9E3779B9u; }

template <int T> __device__ __forceinline__ u32x4 frag(unsigned pk, const u32x4& sel) {
  const unsigned z = dppu<BCAST + 2 * T>(pk);
  u32x4 b;
#pragma unroll
  for (int w = 0; w < 4; ++w) b[w] = __builtin_amdgcn_perm(z, z, sel[w]);
  return b;
}
__device__ __forceinline__ void mma(f32x4& acc, const u32x4& a, const u32x4& v) {
  acc = __builtin_amdgcn_mfma_f32_16x16x32_bf16(__builtin_bit_cast(bf16x8, a), __builtin_bit_cast(bf16x8, v), acc, 0, 0, 0);
}

__global__ void __launch_bounds__(NW * 64, 2) gqa_decode(const unsigned char* __restrict__ q, const unsigned char* __restrict__ k,
                                                        const unsigned char* __restrict__ v, unsigned char* ws,
                                                        unsigned short* out) {
  __shared__ __attribute__((aligned(16))) float lds[NW * 256 + 20];
  const int tid = threadIdx.x, lane = tid & 63, wave = __builtin_amdgcn_readfirstlane(tid >> 6);
  const int c = lane & 15;
  if (__builtin_expect(blockIdx.x >= NGRP * 4, 0)) {
    const unsigned g8 = (blockIdx.x & 7) * 8;
    const auto r = __builtin_amdgcn_make_buffer_rsrc(ws, 0, NGRP * GRP_BYTES, 0x00020000);
    const unsigned eo = tid < 8 ? (g8 + tid) * GRP_BYTES + WS_EP : OOB;
    const unsigned ep = __builtin_amdgcn_raw_buffer_load_b32(r, eo, 0, 16);
    unsigned best[2] = {~0u, ~0u};
    _Pragma("unroll 1") for (int i = 0; i < NPR; ++i) {
#pragma unroll
      for (int z = 0; z < 2; ++z) {
        const unsigned char* a = ws + (g8 + wave * 2 + (i & 1)) * GRP_BYTES + z * 8192 + WS_PR + (i >> 1) * 256;
        unsigned long long t0, t1;
        unsigned d;
        asm volatile("s_memtime %0\n\ts_waitcnt lgkmcnt(0)\n\ts_load_dword %2, %3, 0x0\n\ts_waitcnt lgkmcnt(0)\n\ts_memtime %1\n\ts_waitcnt lgkmcnt(0)"
                     : "=&s"(t0), "=&s"(t1), "=&s"(d) : "s"(a) : "memory");
        best[z] = min(best[z], (unsigned)t1 - (unsigned)t0);
      }
    }
    unsigned* const lb = reinterpret_cast<unsigned*>(lds);
    lb[wave] = best[0]; lb[4 + wave] = best[1];
    wg_barrier();
    const unsigned b0 = min(min(lb[0], lb[1]), min(lb[2], lb[3])), b1 = min(min(lb[4], lb[5]), min(lb[6], lb[7]));
    __builtin_amdgcn_raw_buffer_store_b32(mktag(ep) + 2 * (b1 < b0), r, eo + 8, 0, 16);
    return;
  }
  const int grp = (blockIdx.x & 7) * 8 + (blockIdx.x >> 5), sp = (blockIdx.x >> 3) & 3;
  const unsigned vb = lane * 16 + wave * 4096 + grp * 1048576, ct = (grp & 15) << 14;
  unsigned vo[4];
#pragma unroll
  for (int x = 0; x < 4; ++x) vo[x] = vb + ((x ^ sp) << 10);
  unsigned qo = c * 16 + grp * 512;
  asm volatile("" : "+v"(vo[0]), "+v"(vo[1]), "+v"(vo[2]), "+v"(vo[3]), "+v"(qo));
  SB();
  const auto rk = __builtin_amdgcn_make_buffer_rsrc((void*)k, 0, 64u << 20, 0x00020000);
  u32x4 R[WIN], q0, q1;
  const int h = lane >> 5;
  unsigned oo, g0, gb, gi, tag, tb;
  unsigned short o0;
  unsigned cp, tp, g2, cv = ct;
  __amdgpu_buffer_rsrc_t rv, rw, ro;
  auto issue = [&](auto ic) {
    constexpr int I = decltype(ic)::value;
    if constexpr (I >= 2 && I % 2 == 0) {
      UNTIL(due(I));
      SB();
    }
    if constexpr (I < 64) R[I % WIN] = __builtin_amdgcn_raw_buffer_load_b128(rk, vo[quad(I)], (I * 16384) ^ ct, 3);
    else R[I % WIN] = __builtin_amdgcn_raw_buffer_load_b128(rv, vo[quad(I)], ((I - 64) * 16384) ^ cv, 3);
    SB();
    if constexpr (I == 1) {
      const unsigned char* tpp = sp == 0 ? v + grp * 1048576 : sp == 1 ? ws + grp * GRP_BYTES : (const unsigned char*)out + grp * 512;
      tp = __builtin_amdgcn_raw_buffer_load_b32(__builtin_amdgcn_make_buffer_rsrc((void*)tpp, 0, wave ? 0 : 4, 0x00020000), 0, 0, 0);
      const auto rq = __builtin_amdgcn_make_buffer_rsrc((void*)q, 0, 32768, 0x00020000);
      q0 = __builtin_amdgcn_raw_buffer_load_b128(rq, qo, 0, 0);
      q1 = __builtin_amdgcn_raw_buffer_load_b128(rq, qo + 256, 0, 0);
      unsigned long long pc;
      unsigned len;
      asm volatile("s_getpc_b64 %0\n\ts_mov_b32 %1, .Lgqa_end-." : "=s"(pc), "=s"(len));
      cp = __builtin_amdgcn_raw_buffer_load_b32(__builtin_amdgcn_make_buffer_rsrc((void*)pc, 0, len, 0x00020000), ((blockIdx.x >> 3) * 4 + wave) * 128 + 256, 0, 0);
      rv = __builtin_amdgcn_make_buffer_rsrc((void*)v, 0, 64u << 20, 0x00020000);
      rw = __builtin_amdgcn_make_buffer_rsrc(ws, 0, NGRP * GRP_BYTES, 0x00020000);
      ro = __builtin_amdgcn_make_buffer_rsrc(out, 0, 32768, 0x00020000);
      gb = grp * GRP_BYTES;
      SB();
      unsigned long long t64 = __builtin_amdgcn_s_memrealtime();
      SB();
      asm volatile("" : "+s"(t64), "+s"(cv));
      tb = (unsigned)t64;
      oo = (grp * 256 + h * 128 + sp * 32 + (lane & 31)) * 2;
    }
    if constexpr (I == PW2) {
      CB();
      g2 = __builtin_amdgcn_raw_buffer_load_b32(rw, lane < 3 ? sp * 1536 + (wave * 3 + lane) * 128 : OOB, gi, 16);
      CB();
      SB();
    }
    if constexpr (I == PW) {
      CB();
      const unsigned skip = wave == sp ? 0u : OOB;
      g0 = __builtin_amdgcn_raw_buffer_load_b32(rw, (lane < 12 ? OOB : lane < 14 ? WS_ML + (lane - 12) * 128 : lane == 14 ? WS_EP + 8 : OOB) | skip, gb, 16);
      o0 = __builtin_amdgcn_raw_buffer_load_b16(ro, oo | skip, 0, 16);
      CB();
      SB();
    }
  };
  sfor<nis(-1)>(issue);
  auto step = [&](auto ic) { constexpr int I = decltype(ic)::value; sfor<nis(I) - nis(I - 1)>([&](auto dc) { issue(std::integral_constant<int, nis(I - 1) + decltype(dc)::value>{}); }); };
  float s[8];
  sfor<8>([&](auto gc) {
    constexpr int G = decltype(gc)::value;
    float x[16];
    sfor<8>([&](auto tc) {
      constexpr int T = decltype(tc)::value, I = G * 8 + T;
      float d0 = 0.f, d1 = 0.f;
#pragma unroll
      for (int w = 0; w < 4; ++w) { d0 = dot2(R[I % WIN][w], q0[w], d0); d1 = dot2(R[I % WIN][w], q1[w], d1); }
      x[2 * T] = d0; x[2 * T + 1] = d1;
      SB();
      step(std::integral_constant<int, I>{});
    });
#define TR_STEP(HALF, CTRL, BIT) _Pragma("unroll") for (int h = 0; h < (HALF); ++h) { \
      const float lo_ = x[h], hi_ = x[h + (HALF)]; const float t1_ = lo_ + dpp<CTRL>(lo_), t2_ = hi_ + dpp<CTRL>(hi_); \
      x[h] = (c & (BIT)) ? t2_ : t1_; }
    TR_STEP(8, XOR8, 8) TR_STEP(4, XOR7, 4) TR_STEP(2, XOR2, 2) TR_STEP(1, XOR1, 1)
#undef TR_STEP
    s[G] = x[0] * C2;
    SB();
  });
  asm volatile("" :: "v"(cp), "v"(tp));
  float m = s[0];
#pragma unroll
  for (int g = 1; g < 8; ++g) m = fmaxf(m, s[g]);
  m = fmaxf(m, dpp<XOR2>(m)); m = fmaxf(m, dpp<ROR4>(m)); m = fmaxf(m, dpp<XOR8>(m)); m = xrow_max(m);
  float l = 0.f;
  unsigned pk[8];
#pragma unroll
  for (int g = 0; g < 8; ++g) {
    const float p = ex2(s[g] - m);
    l += p;
    pk[g] = cvtpk(p, dpp<XOR1>(p));
  }
  u32x4 sel;
  {
    const int h = c >> 3, j = c & 7;
    const unsigned srcb = h ? 0x0302u : 0x0100u;
    const unsigned pat = (j & 1) ? ((srcb << 16) | 0x0c0cu) : (0x0c0c0000u | srcb);
#pragma unroll
    for (int w = 0; w < 4; ++w) sel[w] = ((j >> 1) == w) ? pat : 0x0c0c0c0cu;
  }
  SB();
  f32x4 acc = {0.f, 0.f, 0.f, 0.f};
  float* const lml = lds + NW * 256;
  float ew;
  unsigned lw, lr, xo, mo, eo;
  u32x4 gm;
  sfor<64>([&](auto jc) {
    constexpr int J = decltype(jc)::value, I = 64 + J;
    if constexpr (I == BAR1) {
      wg_barrier();
      const f32x4 a = *reinterpret_cast<const f32x4*>(lml + h * 8), b = *reinterpret_cast<const f32x4*>(lml + h * 8 + 4);
      const float mw = lml[h * 8 + wave];
      tag = mktag(__float_as_uint(lml[16]));
      unsigned sd = __builtin_amdgcn_readfirstlane(__float_as_uint(lml[17]) - tag);
      if (__builtin_expect(sd & ~2u, 0)) {
        _Pragma("unroll 1") for (unsigned spin = 0; spin < (1u << 22); ++spin) {
          CB();
          sd = __builtin_amdgcn_readfirstlane(__builtin_amdgcn_raw_buffer_load_b32(rw, WS_EP + 8, gb, 16) - tag);
          if (!(sd & ~2u)) break;
        }
      }
      gi = gb + ((sd & 2) << 12);
      const float M = fmaxf(fmaxf(a[0], a[1]), fmaxf(a[2], a[3]));
      const float L = fmaf(ex2(a[3] - M), b[3], fmaf(ex2(a[2] - M), b[2], fmaf(ex2(a[1] - M), b[1], ex2(a[0] - M) * b[0])));
      ew = ex2(mw - M);
      __builtin_amdgcn_raw_buffer_store_b128(u32x4{tag, __float_as_uint(M), tag, __float_as_uint(L)}, rw,
                                             ((lane & 31) || wave != sp) ? OOB : WS_ML + sp * 32 + h * 16, gb, 16);
      lw = wave * 1024 + lane * 16;
      if (wave != sp) {
        lr = (((lane & 16) * 2 + (lane & 2) * 8 + wave * 4 + ((lane >> 2) & 3)) * 4 + (lane & 1) * 2) * 4;
        xo = lane < 32 ? wave * 1536 + ((sp - wave - 1) & 3) * 512 + lane * 16 : OOB;
      } else {
        lr = ((h * 32 + (lane & 4) * 4 + wave * 4 + ((lane >> 3) & 3)) * 4 + (lane & 3)) * 4;
        xo = sp * 1536 + lane * 8;
      }
      mo = wave == sp ? WS_ML + ((sp + 1 + lane) & 3) * 32 + h * 16 : OOB;
      eo = (lane | sp) ? OOB : WS_EP;
      asm volatile("" : "+v"(ew), "+v"(tag), "+v"(lw), "+v"(lr), "+v"(xo), "+v"(mo), "+v"(eo));
      SB();
    }
    mma(acc, frag<J & 7>(pk[J >> 3], sel), R[I % WIN]);
    SB();
    step(std::integral_constant<int, I>{});
    if constexpr (I == iall()) { CB(); gm = __builtin_amdgcn_raw_buffer_load_b128(rw, mo, gb, 16); CB(); }
    if constexpr (I == 64 + WIN) {
      l += dpp<XOR2>(l); l += dpp<ROR4>(l); l += dpp<XOR8>(l); l = xrow_sum(l);
      if (lane < 2) { lml[lane * 8 + wave] = m; lml[lane * 8 + 4 + wave] = l; }
    }
    if constexpr (I == PW + 1) {
      asm volatile("" :: "v"(o0));
      lml[(wave == sp && (lane == 13 || lane == 14)) ? 3 + lane : 18] = __uint_as_float(g0);
    }
    SB();
  });
  *reinterpret_cast<f32x4*>(reinterpret_cast<char*>(lds) + lw) = acc * ew;
  asm volatile("" :: "v"(g2));
  wg_barrier();
  const float* src = reinterpret_cast<const float*>(reinterpret_cast<const char*>(lds) + lr);
  if (wave != sp) {
    const f32x2 o = (*(const f32x2*)src + *(const f32x2*)(src + 256)) + (*(const f32x2*)(src + 512) + *(const f32x2*)(src + 768));
    __builtin_amdgcn_raw_buffer_store_b128(u32x4{tag, __float_as_uint(o[0]), tag, __float_as_uint(o[1])}, rw, xo, gi, 16);
    asm volatile("s_endpgm" ::: "memory");
  } else {
    u32x2 g[3];
    auto ldv = [&]() {
#pragma unroll
      for (int j = 0; j < 3; ++j) g[j] = __builtin_amdgcn_raw_buffer_load_b64(rw, xo + j * 512, gi, 16);
    };
    const float oe = (src[0] + src[256]) + (src[512] + src[768]);
    if (!ALL((gm[0] == tag) & (gm[2] == tag))) {
      _Pragma("unroll 1") for (unsigned spin = 0; spin < (1u << 22); ++spin) {
        CB();
        gm = __builtin_amdgcn_raw_buffer_load_b128(rw, mo, gb, 16);
        if (ALL((gm[0] == tag) & (gm[2] == tag))) break;
      }
    }
    __builtin_amdgcn_raw_buffer_store_b32(tag, rw, eo, gb, 16);
    CB();
    const float Mj = __uint_as_float(gm[1]);
    float M = fmaxf(Mj, dpp<XOR1>(Mj)); M = fmaxf(M, dpp<XOR2>(M));
    const float e = ex2(Mj - M);
    float L = e * __uint_as_float(gm[3]); L += dpp<XOR1>(L); L += dpp<XOR2>(L);
    const float w = e * __builtin_amdgcn_rcpf(L);
    float w0 = dpp<0x00>(w), w1 = dpp<0x55>(w), w2 = dpp<0xAA>(w), o = dpp<0xFF>(w) * oe;
    asm volatile("" : "+v"(w0), "+v"(w1), "+v"(w2), "+v"(o));
    SB();
    UNTIL(due(128) + TPOLL / 10);
    CB();
    ldv();
    if (!ALL((g[0][0] == tag) & (g[1][0] == tag) & (g[2][0] == tag))) {
      _Pragma("unroll 1") for (unsigned spin = 0; spin < (1u << 22); ++spin) {
        CB();
        ldv();
        if (ALL((g[0][0] == tag) & (g[1][0] == tag) & (g[2][0] == tag))) break;
      }
    }
    o = fmaf(w2, __uint_as_float(g[2][1]), fmaf(w1, __uint_as_float(g[1][1]), fmaf(w0, __uint_as_float(g[0][1]), o)));
    __builtin_amdgcn_raw_buffer_store_b16(__builtin_bit_cast(unsigned short, (__bf16)o), ro, oo, 0, 16);
    SB();
    asm volatile(".Lgqa_end:\n\ts_endpgm" ::: "memory");
  }
}

extern "C" void kernel_launch(void* const* d_in, const int* in_sizes, int n_in, void* d_out, int out_size, void* d_ws,
                              size_t ws_size, hipStream_t stream) {
  gqa_decode<<<NGRP * 4 + 8, NW * 64, 0, stream>>>((const unsigned char*)d_in[0], (const unsigned char*)d_in[1], (const unsigned char*)d_in[2], (unsigned char*)d_ws, (unsigned short*)d_out);
}
```
